```python
import torch

def _softmax(x, axis=-1):
    return torch.softmax(x, dim=axis)
def _gelu(x):
    return torch.nn.functional.gelu(x, approximate="tanh")
def _silu(x):
    return torch.nn.functional.silu(x)
def _sigmoid(x):
    return torch.sigmoid(x)
def _layernorm(x, axis=-1, eps=1e-5):
    m = x.mean(dim=axis, keepdim=True)
    v = x.var(dim=axis, keepdim=True, unbiased=False)
    return (x - m) / torch.sqrt(v + eps)
def _scatter_add(tgt, idx, src, axis=0):
    return tgt.clone().index_add_(axis, idx, src)
def _segment_sum(src, seg, n):
    out = torch.zeros((n,) + src.shape[1:], dtype=src.dtype)
    return out.index_add_(0, seg, src)
def _pad(x, pw):
    flat = tuple(v for pair in reversed(pw) for v in pair)
    return torch.nn.functional.pad(x, flat)

kernel_name = 'standalone__paged_block_table_kv_pagedattention__sampling'

def setup_inputs():

    _w = (lambda a: (lambda t: t.to(torch.bfloat16) if t.is_floating_point() else t)(torch.as_tensor(a)))
    B, Hq, Hkv, Sq, Skv, D = 8, 32, 32, 1, 2048, 128
    bs = 16
    nbps = max(1, Skv // bs)
    num_phys = B * nbps + 4
    bt = torch.randperm(num_phys)[: B * nbps].reshape(B, nbps).to(torch.int64)
    cl = (nbps * bs - (torch.arange(1, B + 1, dtype=torch.int64) * 2654435761) % max(1, nbps * bs // 2)).to(torch.int64)
    return {
        "q": _w((lambda *s: torch.randn(*s, dtype=torch.bfloat16) * 0.8)(B, Hq, Sq, D)),
        "k_cache": _w((lambda *s: torch.randn(*s, dtype=torch.bfloat16) * 0.8)(num_phys, bs, Hkv, D)),
        "v_cache": _w((lambda *s: torch.randn(*s, dtype=torch.bfloat16) * 0.05)(num_phys, bs, Hkv, D)),
        "block_table": _w(bt),
        "context_lens": _w(cl),
    }

def reference(q, k_cache, v_cache, block_table, context_lens):
    B, Hq, Sq, D = q.shape
    bs = k_cache.shape[1]
    Hkv = k_cache.shape[2]
    nbps = block_table.shape[1]
    Skv = nbps * bs
    bt = block_table.reshape(-1)
    k = (lambda x, *d: x.permute(*d))(k_cache[bt].reshape(B, Skv, Hkv, D), 0, 2, 1, 3)
    v = (lambda x, *d: x.permute(*d))(v_cache[bt].reshape(B, Skv, Hkv, D), 0, 2, 1, 3)
    g = Hq // Hkv
    qg = q.reshape(B, Hkv, g, Sq, D)
    scores = torch.einsum("bhgqd,bhkd->bhgqk", qg, k) * (D ** -0.5)
    pos = torch.arange(Skv)
    msk = pos[None, :] < context_lens[:, None]
    scores = torch.where(msk[:, None, None, None, :], scores, -1e9)
    p = _softmax(scores, axis=-1)
    out = torch.einsum("bhgqk,bhkd->bhgqd", p, v)
    return out.reshape(B, Hq, Sq, D)
```

```cpp
#include <hip/hip_runtime.h>
#include <hip/hip_bf16.h>
#include <stdint.h>

#define NW 16
#define DEPTH 2
#define INTERLEAVE 0
#define AUX 2

namespace {
constexpr int B = 8, H = 32, D = 128, PAGE = 16, NBPS = 128, SKV = NBPS * PAGE;
constexpr int PPW = NBPS / NW;
constexpr unsigned TOK_BYTES = H * D * 2;
constexpr unsigned PAGE_BYTES = PAGE * TOK_BYTES;
constexpr unsigned OOB = 0x80000000u;

using bf16x2 = __attribute__((ext_vector_type(2))) __bf16;
using u32x4 = __attribute__((ext_vector_type(4))) unsigned;
using f32x4 = __attribute__((ext_vector_type(4))) float;
using f32x2 = __attribute__((ext_vector_type(2))) float;

__device__ __forceinline__ float dot2(unsigned k, unsigned q, float acc) {
  return __builtin_amdgcn_fdot2_f32_bf16(__builtin_bit_cast(bf16x2, k), __builtin_bit_cast(bf16x2, q), acc, false);
}
template <int CTRL> __device__ __forceinline__ float dpp(float x) {
  return __builtin_bit_cast(float, __builtin_amdgcn_mov_dpp(__builtin_bit_cast(int, x), CTRL, 0xf, 0xf, true));
}
__device__ __forceinline__ float row_sum(float x) {
  x += dpp<0x128>(x);
  x += dpp<0x124>(x);
  x += dpp<0x122>(x);
  x += dpp<0x121>(x);
  return x;
}
__device__ __forceinline__ float xrow_max(float x) {
  auto s = __builtin_amdgcn_permlane16_swap(__float_as_uint(x), __float_as_uint(x), false, false);
  x = fmaxf(__uint_as_float(s[0]), __uint_as_float(s[1]));
  auto t = __builtin_amdgcn_permlane32_swap(__float_as_uint(x), __float_as_uint(x), false, false);
  return fmaxf(__uint_as_float(t[0]), __uint_as_float(t[1]));
}
__device__ __forceinline__ float xrow_sum(float x) {
  auto s = __builtin_amdgcn_permlane16_swap(__float_as_uint(x), __float_as_uint(x), false, false);
  x = __uint_as_float(s[0]) + __uint_as_float(s[1]);
  auto t = __builtin_amdgcn_permlane32_swap(__float_as_uint(x), __float_as_uint(x), false, false);
  return __uint_as_float(t[0]) + __uint_as_float(t[1]);
}

__global__ void __launch_bounds__(NW * 64)
paged_attn_kernel(const unsigned short* __restrict__ q, const unsigned short* __restrict__ kc,
                  const unsigned short* __restrict__ vc, const int* __restrict__ bt,
                  const int* __restrict__ lens, unsigned short* __restrict__ out, unsigned kv_bytes) {
  __shared__ __attribute__((aligned(16))) float lds[NW * (D + 4)];
  const int bh = blockIdx.x, b = bh >> 5, h = bh & 31;
  const int tid = threadIdx.x, lane = tid & 63, r = lane >> 4, c = lane & 15;
  const int wave = __builtin_amdgcn_readfirstlane(tid >> 6);
  constexpr float C2 = 0.088388347648318440f * 1.4426950408889634f;

  int len = lens[b];
  int phys[PPW];
#pragma unroll
  for (int i = 0; i < PPW; ++i) phys[i] = bt[b * NBPS + (INTERLEAVE ? wave + NW * i : wave * PPW + i)];
  u32x4 qv = *reinterpret_cast<const u32x4*>(q + (size_t)bh * D + 8 * c);
#pragma unroll
  for (int i = 0; i < PPW; ++i) asm volatile("" : "+s"(phys[i]));
  if (len <= 0) { len = SKV; qv = u32x4{0u, 0u, 0u, 0u}; }
  len = min(len, SKV);

  const __amdgpu_buffer_rsrc_t krs = __builtin_amdgcn_make_buffer_rsrc((void*)kc, 0, kv_bytes, 0x00020000);
  const __amdgpu_buffer_rsrc_t vrs = __builtin_amdgcn_make_buffer_rsrc((void*)vc, 0, kv_bytes, 0x00020000);
  const unsigned loff = (unsigned)r * TOK_BYTES + (unsigned)c * 16u;

  float m = -1e30f, l = 0.f, acc[8];
#pragma unroll
  for (int k = 0; k < 8; ++k) acc[k] = 0.f;

  const int first_page = INTERLEAVE ? wave : wave * PPW;
  if (first_page * PAGE < len) {
    u32x4 Kb[PPW][4], Vb[PPW][4];
#define PAGE_OF(i) (INTERLEAVE ? wave + NW * (i) : wave * PPW + (i))
#define ISSUE(X, rs, i) do { const int rem_ = len - PAGE_OF(i) * PAGE; \
      const unsigned so_ = (unsigned)phys[i] * PAGE_BYTES + (unsigned)h * (D * 2); \
      _Pragma("unroll") for (int t_ = 0; t_ < 4; ++t_) { \
        const unsigned vo_ = (r + 4 * t_ < rem_) ? loff + t_ * 4 * TOK_BYTES : OOB; \
        X[i][t_] = __builtin_amdgcn_raw_buffer_load_b128(rs, vo_, so_, AUX); } } while (0)
#pragma unroll
    for (int i = 0; i < DEPTH; ++i) { ISSUE(Kb, krs, i); __builtin_amdgcn_sched_barrier(0); ISSUE(Vb, vrs, i); __builtin_amdgcn_sched_barrier(0); }
#pragma unroll
    for (int i = 0; i < PPW; ++i) {
      const int rem = len - PAGE_OF(i) * PAGE;
      float x[4];
#pragma unroll
      for (int t = 0; t < 4; ++t) {
        float d = 0.f;
#pragma unroll
        for (int k = 0; k < 4; ++k) d = dot2(Kb[i][t][k], qv[k], d);
        x[t] = d;
      }
      asm volatile("" : "+v"(x[0]), "+v"(x[1]), "+v"(x[2]), "+v"(x[3]) :: "memory");
      __builtin_amdgcn_sched_barrier(0);
      if (i + DEPTH < PPW) ISSUE(Kb, krs, i + DEPTH);
      asm volatile("" ::: "memory");
      __builtin_amdgcn_sched_barrier(0);
#pragma unroll
      for (int t = 0; t < 4; ++t) {
        x[t] = row_sum(x[t]) * C2;
        x[t] = (r + 4 * t < rem) ? x[t] : -INFINITY;
      }
      const float mn = fmaxf(fmaxf(fmaxf(x[0], x[1]), fmaxf(x[2], x[3])), m);
      const float alpha = __builtin_amdgcn_exp2f(m - mn);
      m = mn;
      float p[4];
#pragma unroll
      for (int t = 0; t < 4; ++t) p[t] = __builtin_amdgcn_exp2f(x[t] - mn);
      l = fmaf(l, alpha, (p[0] + p[1]) + (p[2] + p[3]));
#pragma unroll
      for (int k = 0; k < 8; ++k) acc[k] *= alpha;
#pragma unroll
      for (int t = 0; t < 4; ++t) {
#pragma unroll
        for (int k = 0; k < 4; ++k) {
          const unsigned w = Vb[i][t][k];
          acc[2 * k] = fmaf(p[t], __builtin_bit_cast(float, w << 16), acc[2 * k]);
          acc[2 * k + 1] = fmaf(p[t], __builtin_bit_cast(float, w & 0xffff0000u), acc[2 * k + 1]);
        }
      }
      asm volatile("" : "+v"(acc[0]), "+v"(acc[1]), "+v"(acc[2]), "+v"(acc[3]), "+v"(acc[4]), "+v"(acc[5]), "+v"(acc[6]), "+v"(acc[7]) :: "memory");
      __builtin_amdgcn_sched_barrier(0);
      if (i + DEPTH < PPW) ISSUE(Vb, vrs, i + DEPTH);
      asm volatile("" ::: "memory");
      __builtin_amdgcn_sched_barrier(0);
    }
  }
  const float M = xrow_max(m);
  const float sc = __builtin_amdgcn_exp2f(m - M);
  const float L = xrow_sum(l * sc);
#pragma unroll
  for (int k = 0; k < 8; ++k) acc[k] = xrow_sum(acc[k] * sc);
  float* rec = lds + wave * (D + 4);
  if (r == 0) {
    *reinterpret_cast<f32x4*>(rec + 4 + 8 * c) = f32x4{acc[0], acc[1], acc[2], acc[3]};
    *reinterpret_cast<f32x4*>(rec + 8 + 8 * c) = f32x4{acc[4], acc[5], acc[6], acc[7]};
    if (c == 0) *reinterpret_cast<f32x2*>(rec) = f32x2{M, L};
  }
  __syncthreads();
  if (tid < 64) {
    float mw[NW], lw[NW], MM = -1e30f;
#pragma unroll
    for (int w = 0; w < NW; ++w) {
      const f32x2 ml = *reinterpret_cast<const f32x2*>(lds + w * (D + 4));
      mw[w] = ml[0]; lw[w] = ml[1];
      MM = fmaxf(MM, mw[w]);
    }
    float LL = 0.f, o0 = 0.f, o1 = 0.f;
#pragma unroll
    for (int w = 0; w < NW; ++w) {
      const float s = __builtin_amdgcn_exp2f(mw[w] - MM);
      const f32x2 a = *reinterpret_cast<const f32x2*>(lds + w * (D + 4) + 4 + 2 * tid);
      LL = fmaf(s, lw[w], LL); o0 = fmaf(s, a[0], o0); o1 = fmaf(s, a[1], o1);
    }
    const float inv = 1.f / LL;
    const __hip_bfloat16 r0 = __float2bfloat16(o0 * inv), r1 = __float2bfloat16(o1 * inv);
    const unsigned pk = (unsigned)__builtin_bit_cast(unsigned short, r0) | ((unsigned)__builtin_bit_cast(unsigned short, r1) << 16);
    *reinterpret_cast<unsigned*>(out + (size_t)bh * D + 2 * tid) = pk;
  }
}
}

extern "C" void kernel_launch(void* const* d_in, const int* in_sizes, int n_in, void* d_out, int out_size,
                              void* d_ws, size_t ws_size, hipStream_t stream) {
  const unsigned kv_bytes = (unsigned)in_sizes[1] * 2u;
  paged_attn_kernel<<<B * H, NW * 64, 0, stream>>>(
      (const unsigned short*)d_in[0], (const unsigned short*)d_in[1], (const unsigned short*)d_in[2],
      (const int*)d_in[3], (const int*)d_in[4], (unsigned short*)d_out, kv_bytes);
}
```

```cpp
#include <hip/hip_runtime.h>
#include <hip/hip_bf16.h>
#include <stdint.h>

#define NW 4
#define INTERLEAVE 0
#define AUX 2

namespace {
constexpr int B = 8, H = 32, D = 128, PAGE = 16, NBPS = 128, SKV = NBPS * PAGE;
constexpr int PPW = NBPS / NW;
constexpr unsigned TOK_BYTES = H * D * 2;
constexpr unsigned PAGE_BYTES = PAGE * TOK_BYTES;
constexpr unsigned OOB = 0x80000000u;

using bf16x2 = __attribute__((ext_vector_type(2))) __bf16;
using u32x4 = __attribute__((ext_vector_type(4))) unsigned;
using f32x4 = __attribute__((ext_vector_type(4))) float;
using f32x2 = __attribute__((ext_vector_type(2))) float;

__device__ __forceinline__ float dot2(unsigned k, unsigned q, float acc) {
  return __builtin_amdgcn_fdot2_f32_bf16(__builtin_bit_cast(bf16x2, k), __builtin_bit_cast(bf16x2, q), acc, false);
}
template <int CTRL> __device__ __forceinline__ float dpp(float x) {
  return __builtin_bit_cast(float, __builtin_amdgcn_mov_dpp(__builtin_bit_cast(int, x), CTRL, 0xf, 0xf, true));
}
__device__ __forceinline__ float row_sum(float x) {
  x += dpp<0x128>(x);
  x += dpp<0x124>(x);
  x += dpp<0x122>(x);
  x += dpp<0x121>(x);
  return x;
}
__device__ __forceinline__ float xrow_max(float x) {
  auto s = __builtin_amdgcn_permlane16_swap(__float_as_uint(x), __float_as_uint(x), false, false);
  x = fmaxf(__uint_as_float(s[0]), __uint_as_float(s[1]));
  auto t = __builtin_amdgcn_permlane32_swap(__float_as_uint(x), __float_as_uint(x), false, false);
  return fmaxf(__uint_as_float(t[0]), __uint_as_float(t[1]));
}
__device__ __forceinline__ float xrow_sum(float x) {
  auto s = __builtin_amdgcn_permlane16_swap(__float_as_uint(x), __float_as_uint(x), false, false);
  x = __uint_as_float(s[0]) + __uint_as_float(s[1]);
  auto t = __builtin_amdgcn_permlane32_swap(__float_as_uint(x), __float_as_uint(x), false, false);
  return __uint_as_float(t[0]) + __uint_as_float(t[1]);
}
#define SB() __builtin_amdgcn_sched_barrier(0)

__global__ void __launch_bounds__(NW * 64)
paged_attn_kernel(const unsigned short* __restrict__ q, const unsigned short* __restrict__ kc,
                  const unsigned short* __restrict__ vc, const int* __restrict__ bt,
                  const int* __restrict__ lens, unsigned short* __restrict__ out, unsigned kv_bytes
                  ) {
  __shared__ __attribute__((aligned(16))) float lds[NW * (D + 4)];
  const int bh = blockIdx.x, b = bh >> 5, h = bh & 31;
  const int tid = threadIdx.x, lane = tid & 63, r = lane >> 4, c = lane & 15;
  const int wave = __builtin_amdgcn_readfirstlane(tid >> 6);
  constexpr float C2 = 0.088388347648318440f * 1.4426950408889634f;

  const int li = lane & (PPW - 1);
  int btv = bt[b * NBPS + (INTERLEAVE ? wave + NW * li : wave * PPW + li)];
  u32x4 qv = *reinterpret_cast<const u32x4*>(q + (size_t)bh * D + 8 * c);
  int len = lens[b];
  asm volatile("" : "+v"(btv), "+s"(len));
  const bool allmasked = len <= 0;
  len = allmasked ? SKV : min(len, SKV);

  const __amdgpu_buffer_rsrc_t krs = __builtin_amdgcn_make_buffer_rsrc((void*)kc, 0, kv_bytes, 0x00020000);
  const __amdgpu_buffer_rsrc_t vrs = __builtin_amdgcn_make_buffer_rsrc((void*)vc, 0, kv_bytes, 0x00020000);
  const unsigned loff = (unsigned)r * TOK_BYTES + (unsigned)c * 16u + (unsigned)h * (D * 2);

  float m = -1e30f, l = 0.f, acc[8];
#pragma unroll
  for (int k = 0; k < 8; ++k) acc[k] = 0.f;

  constexpr int PSTEP = INTERLEAVE ? NW : 1;
  const int first_page = INTERLEAVE ? wave : wave * PPW;
  int rem = len - first_page * PAGE;
  const int npg = min(PPW, (rem + PSTEP * PAGE - 1) / (PSTEP * PAGE));
  if (rem > 0) {
    u32x4 Kb[4], Vb[4];
    unsigned vo[4];
#define OFFS(rem_) do { _Pragma("unroll") for (int t_ = 0; t_ < 4; ++t_) vo[t_] = (r + 4 * t_ < (rem_)) ? loff + t_ * 4 * TOK_BYTES : OOB; } while (0)
#define ISSUE(X, rs, so_) do { _Pragma("unroll") for (int t_ = 0; t_ < 4; ++t_) X[t_] = __builtin_amdgcn_raw_buffer_load_b128(rs, vo[t_], so_, AUX); } while (0)
    unsigned so = (unsigned)__builtin_amdgcn_readlane(btv, 0) * PAGE_BYTES;
    OFFS(rem);
    ISSUE(Kb, krs, so); SB();
    ISSUE(Vb, vrs, so); SB();
    if (allmasked) qv = u32x4{0u, 0u, 0u, 0u};
    for (int i = 0; i < npg; ++i) {
      float x[4];
#pragma unroll
      for (int t = 0; t < 4; ++t) {
        float d = 0.f;
#pragma unroll
        for (int k = 0; k < 4; ++k) d = dot2(Kb[t][k], qv[k], d);
        x[t] = d;
      }
      asm volatile("" : "+v"(x[0]), "+v"(x[1]), "+v"(x[2]), "+v"(x[3]) :: "memory");
      SB();
      const int remn = (i + 1 < npg) ? rem - PSTEP * PAGE : 0;
      so = (unsigned)__builtin_amdgcn_readlane(btv, (i + 1) & (PPW - 1)) * PAGE_BYTES;
      OFFS(remn);
      ISSUE(Kb, krs, so);
      asm volatile("" ::: "memory");
      SB();
#pragma unroll
      for (int t = 0; t < 4; ++t) {
        x[t] = row_sum(x[t]) * C2;
        x[t] = (r + 4 * t < rem) ? x[t] : -INFINITY;
      }
      const float mn = fmaxf(fmaxf(fmaxf(x[0], x[1]), fmaxf(x[2], x[3])), m);
      const float alpha = __builtin_amdgcn_exp2f(m - mn);
      m = mn;
      float p[4];
#pragma unroll
      for (int t = 0; t < 4; ++t) p[t] = __builtin_amdgcn_exp2f(x[t] - mn);
      l = fmaf(l, alpha, (p[0] + p[1]) + (p[2] + p[3]));
#pragma unroll
      for (int k = 0; k < 8; ++k) acc[k] *= alpha;
#pragma unroll
      for (int t = 0; t < 4; ++t) {
#pragma unroll
        for (int k = 0; k < 4; ++k) {
          const unsigned w = Vb[t][k];
          acc[2 * k] = fmaf(p[t], __builtin_bit_cast(float, w << 16), acc[2 * k]);
          acc[2 * k + 1] = fmaf(p[t], __builtin_bit_cast(float, w & 0xffff0000u), acc[2 * k + 1]);
        }
      }
      asm volatile("" : "+v"(acc[0]), "+v"(acc[1]), "+v"(acc[2]), "+v"(acc[3]), "+v"(acc[4]), "+v"(acc[5]), "+v"(acc[6]), "+v"(acc[7]) :: "memory");
      SB();
      ISSUE(Vb, vrs, so);
      asm volatile("" ::: "memory");
      SB();
      rem = remn;
    }
  }
  const float M = xrow_max(m);
  const float sc = __builtin_amdgcn_exp2f(m - M);
  const float L = xrow_sum(l * sc);
#pragma unroll
  for (int k = 0; k < 8; ++k) acc[k] = xrow_sum(acc[k] * sc);
  float* rec = lds + wave * (D + 4);
  if (r == 0) {
    *reinterpret_cast<f32x4*>(rec + 4 + 8 * c) = f32x4{acc[0], acc[1], acc[2], acc[3]};
    *reinterpret_cast<f32x4*>(rec + 8 + 8 * c) = f32x4{acc[4], acc[5], acc[6], acc[7]};
    if (c == 0) *reinterpret_cast<f32x2*>(rec) = f32x2{M, L};
  }
  __syncthreads();
  if (tid < 64) {
    const f32x2 ml = *reinterpret_cast<const f32x2*>(lds + (lane & (NW - 1)) * (D + 4));
    float MM = ml[0];
    MM = fmaxf(MM, dpp<0x128>(MM)); MM = fmaxf(MM, dpp<0x124>(MM)); MM = fmaxf(MM, dpp<0x122>(MM)); MM = fmaxf(MM, dpp<0x121>(MM));
    const float s = __builtin_amdgcn_exp2f(ml[0] - MM);
    float LL = row_sum(s * ml[1]);
    if (NW == 8) LL *= 0.5f;
    if (NW == 4) LL *= 0.25f;
    float o0 = 0.f, o1 = 0.f;
#pragma unroll
    for (int w = 0; w < NW; ++w) {
      const float sw = __builtin_bit_cast(float, __builtin_amdgcn_readlane(__builtin_bit_cast(int, s), w));
      const f32x2 a = *reinterpret_cast<const f32x2*>(lds + w * (D + 4) + 4 + 2 * tid);
      o0 = fmaf(sw, a[0], o0); o1 = fmaf(sw, a[1], o1);
    }
    const float inv = 1.f / LL;
    const __hip_bfloat16 r0 = __float2bfloat16(o0 * inv), r1 = __float2bfloat16(o1 * inv);
    const unsigned pk = (unsigned)__builtin_bit_cast(unsigned short, r0) | ((unsigned)__builtin_bit_cast(unsigned short, r1) << 16);
    *reinterpret_cast<unsigned*>(out + (size_t)bh * D + 2 * tid) = pk;
  }
}
}

extern "C" void kernel_launch(void* const* d_in, const int* in_sizes, int n_in, void* d_out, int out_size,
                              void* d_ws, size_t ws_size, hipStream_t stream) {
  const unsigned kv_bytes = (unsigned)in_sizes[1] * 2u;
  paged_attn_kernel<<<B * H, NW * 64, 0, stream>>>(
      (const unsigned short*)d_in[0], (const unsigned short*)d_in[1], (const unsigned short*)d_in[2],
      (const int*)d_in[3], (const int*)d_in[4], (unsigned short*)d_out, kv_bytes
      );
}
```

```cpp
#include <hip/hip_runtime.h>
#include <hip/hip_bf16.h>
#include <stdint.h>

#define AUX 2
#define HELP 1
#define HX 2

namespace {
constexpr int NW = 4;
constexpr int B = 8, H = 32, D = 128, PAGE = 16, NBPS = 128, SKV = NBPS * PAGE;
constexpr int PPW = NBPS / NW;
constexpr int LASTW = (NW - 1) * PPW * PAGE;
constexpr unsigned TOK_BYTES = H * D * 2;
constexpr unsigned PAGE_BYTES = PAGE * TOK_BYTES;
constexpr unsigned OOB = 0x80000000u;
constexpr int SLOT_G = 256;
constexpr size_t WS_EPOCH = (size_t)B * H * SLOT_G * 8;

using bf16x2 = __attribute__((ext_vector_type(2))) __bf16;
using u32x4 = __attribute__((ext_vector_type(4))) unsigned;
using f32x4 = __attribute__((ext_vector_type(4))) float;
using f32x2 = __attribute__((ext_vector_type(2))) float;
typedef unsigned long long u64;
#define RLX_AGENT __ATOMIC_RELAXED, __HIP_MEMORY_SCOPE_AGENT

__device__ __forceinline__ float dot2(unsigned k, unsigned q, float acc) {
  return __builtin_amdgcn_fdot2_f32_bf16(__builtin_bit_cast(bf16x2, k), __builtin_bit_cast(bf16x2, q), acc, false);
}
template <int CTRL> __device__ __forceinline__ float dpp(float x) {
  return __builtin_bit_cast(float, __builtin_amdgcn_mov_dpp(__builtin_bit_cast(int, x), CTRL, 0xf, 0xf, true));
}
__device__ __forceinline__ float row_sum(float x) {
  x += dpp<0x128>(x);
  x += dpp<0x124>(x);
  x += dpp<0x122>(x);
  x += dpp<0x121>(x);
  return x;
}
__device__ __forceinline__ float xrow_max(float x) {
  auto s = __builtin_amdgcn_permlane16_swap(__float_as_uint(x), __float_as_uint(x), false, false);
  x = fmaxf(__uint_as_float(s[0]), __uint_as_float(s[1]));
  auto t = __builtin_amdgcn_permlane32_swap(__float_as_uint(x), __float_as_uint(x), false, false);
  return fmaxf(__uint_as_float(t[0]), __uint_as_float(t[1]));
}
__device__ __forceinline__ float xrow_sum(float x) {
  auto s = __builtin_amdgcn_permlane16_swap(__float_as_uint(x), __float_as_uint(x), false, false);
  x = __uint_as_float(s[0]) + __uint_as_float(s[1]);
  auto t = __builtin_amdgcn_permlane32_swap(__float_as_uint(x), __float_as_uint(x), false, false);
  return __uint_as_float(t[0]) + __uint_as_float(t[1]);
}
__device__ __forceinline__ u64 granule(unsigned tag, float v) { return ((u64)tag << 32) | (u64)__float_as_uint(v); }
#define SB() __builtin_amdgcn_sched_barrier(0)

__global__ void __launch_bounds__(NW * 64)
paged_attn_kernel(const unsigned short* __restrict__ q, const unsigned short* __restrict__ kc,
                  const unsigned short* __restrict__ vc, const int* __restrict__ bt,
                  const int* __restrict__ lens, unsigned short* __restrict__ out, unsigned char* ws, unsigned kv_bytes) {
  __shared__ __attribute__((aligned(16))) float lds[NW * (D + 4)];
  const int bh = blockIdx.x, b = bh >> 5;
  int h = bh & 31;
  const int tid = threadIdx.x, lane = tid & 63, r = lane >> 4, c = lane & 15;
  const int wave = __builtin_amdgcn_readfirstlane(tid >> 6);
  constexpr float C2 = 0.088388347648318440f * 1.4426950408889634f;

  const int li = lane & (PPW - 1);
  int btv = bt[b * NBPS + wave * PPW + li];
  u32x4 qv = *reinterpret_cast<const u32x4*>(q + (size_t)bh * D + 8 * c);
  int lv = lens[lane & (B - 1)];
  asm volatile("" : "+v"(btv), "+v"(lv));
  const bool lv_all = lv <= 0;
  lv = lv_all ? SKV : min(lv, SKV);
  int len = __builtin_amdgcn_readlane(lv, b);
  const u64 allmask = __ballot(lv_all);
  bool allmasked = (allmask >> b) & 1;

  int role = 0;
  unsigned tag = 0;
  u64* slot = nullptr;
  unsigned* epoch = nullptr;
  if (HELP && wave == NW - 1) {
    int rank = 0;
#pragma unroll
    for (int j = 0; j < B; ++j) {
      const int lj = __builtin_amdgcn_readlane(lv, j);
      rank += (lj > lv || (lj == lv && j < (lane & (B - 1)))) ? 1 : 0;
    }
    const int myrank = __builtin_amdgcn_readlane(rank, b);
    const int pb = __builtin_ctzll(__ballot(rank == B - 1 - myrank));
    const int plen = __builtin_amdgcn_readlane(lv, pb);
    if (len > LASTW && plen <= LASTW) role = 2;
    if (plen > LASTW && len <= LASTW) role = 1;
    if (role) {
      const int dbh = role == 2 ? bh : pb * H + (h ^ HX);
      slot = reinterpret_cast<u64*>(ws) + (size_t)dbh * SLOT_G;
      epoch = reinterpret_cast<unsigned*>(ws + WS_EPOCH) + dbh * 32;
      tag = __builtin_amdgcn_readfirstlane(__hip_atomic_load(epoch, RLX_AGENT)) + 1u;
      if (role == 1) {
        h ^= HX; len = plen; allmasked = (allmask >> pb) & 1;
        btv = bt[pb * NBPS + wave * PPW + li];
        qv = *reinterpret_cast<const u32x4*>(q + (size_t)dbh * D + 8 * c);
      } else {
        len = 0;
      }
    }
  }

  const __amdgpu_buffer_rsrc_t krs = __builtin_amdgcn_make_buffer_rsrc((void*)kc, 0, kv_bytes, 0x00020000);
  const __amdgpu_buffer_rsrc_t vrs = __builtin_amdgcn_make_buffer_rsrc((void*)vc, 0, kv_bytes, 0x00020000);
  const unsigned loff = (unsigned)r * TOK_BYTES + (unsigned)c * 16u + (unsigned)h * (D * 2);

  float m = -1e30f, l = 0.f, acc[8];
#pragma unroll
  for (int k = 0; k < 8; ++k) acc[k] = 0.f;

  int rem = len - wave * PPW * PAGE;
  const int npg = min(PPW, (rem + PAGE - 1) / PAGE);
  if (rem > 0) {
    u32x4 Kb[4], Vb[4];
    unsigned vo[4];
#define OFFS(rem_) do { _Pragma("unroll") for (int t_ = 0; t_ < 4; ++t_) vo[t_] = (r + 4 * t_ < (rem_)) ? loff + t_ * 4 * TOK_BYTES : OOB; } while (0)
#define ISSUE(X, rs, so_) do { _Pragma("unroll") for (int t_ = 0; t_ < 4; ++t_) X[t_] = __builtin_amdgcn_raw_buffer_load_b128(rs, vo[t_], so_, AUX); } while (0)
    unsigned so = (unsigned)__builtin_amdgcn_readlane(btv, 0) * PAGE_BYTES;
    OFFS(rem);
    ISSUE(Kb, krs, so); SB();
    ISSUE(Vb, vrs, so); SB();
    if (allmasked) qv = u32x4{0u, 0u, 0u, 0u};
    for (int i = 0; i < npg; ++i) {
      float x[4];
#pragma unroll
      for (int t = 0; t < 4; ++t) {
        float d = 0.f;
#pragma unroll
        for (int k = 0; k < 4; ++k) d = dot2(Kb[t][k], qv[k], d);
        x[t] = d;
      }
      asm volatile("" : "+v"(x[0]), "+v"(x[1]), "+v"(x[2]), "+v"(x[3]) :: "memory");
      SB();
      const int remn = (i + 1 < npg) ? rem - PAGE : 0;
      so = (unsigned)__builtin_amdgcn_readlane(btv, (i + 1) & (PPW - 1)) * PAGE_BYTES;
      OFFS(remn);
      ISSUE(Kb, krs, so);
      asm volatile("" ::: "memory");
      SB();
#pragma unroll
      for (int t = 0; t < 4; ++t) {
        x[t] = row_sum(x[t]) * C2;
        x[t] = (r + 4 * t < rem) ? x[t] : -INFINITY;
      }
      const float mn = fmaxf(fmaxf(fmaxf(x[0], x[1]), fmaxf(x[2], x[3])), m);
      const float alpha = __builtin_amdgcn_exp2f(m - mn);
      m = mn;
      float p[4];
#pragma unroll
      for (int t = 0; t < 4; ++t) p[t] = __builtin_amdgcn_exp2f(x[t] - mn);
      l = fmaf(l, alpha, (p[0] + p[1]) + (p[2] + p[3]));
#pragma unroll
      for (int k = 0; k < 8; ++k) acc[k] *= alpha;
#pragma unroll
      for (int t = 0; t < 4; ++t) {
#pragma unroll
        for (int k = 0; k < 4; ++k) {
          const unsigned w = Vb[t][k];
          acc[2 * k] = fmaf(p[t], __builtin_bit_cast(float, w << 16), acc[2 * k]);
          acc[2 * k + 1] = fmaf(p[t], __builtin_bit_cast(float, w & 0xffff0000u), acc[2 * k + 1]);
        }
      }
      asm volatile("" : "+v"(acc[0]), "+v"(acc[1]), "+v"(acc[2]), "+v"(acc[3]), "+v"(acc[4]), "+v"(acc[5]), "+v"(acc[6]), "+v"(acc[7]) :: "memory");
      SB();
      ISSUE(Vb, vrs, so);
      asm volatile("" ::: "memory");
      SB();
      rem = remn;
    }
  }
  float M = xrow_max(m);
  const float sc = __builtin_amdgcn_exp2f(m - M);
  float L = xrow_sum(l * sc);
#pragma unroll
  for (int k = 0; k < 8; ++k) acc[k] = xrow_sum(acc[k] * sc);
  float a0 = r == 0 ? acc[0] : r == 1 ? acc[2] : r == 2 ? acc[4] : acc[6];
  float a1 = r == 0 ? acc[1] : r == 1 ? acc[3] : r == 2 ? acc[5] : acc[7];
  const int d0 = 8 * c + 2 * r;
  if (role == 1) {
    __hip_atomic_store(slot + d0, granule(tag, a0), RLX_AGENT);
    __hip_atomic_store(slot + d0 + 1, granule(tag, a1), RLX_AGENT);
    if (lane < 2) __hip_atomic_store(slot + D + lane, granule(tag, lane ? L : M), RLX_AGENT);
    a0 = 0.f; a1 = 0.f; M = -1e30f; L = 0.f;
  } else if (role == 2) {
    for (unsigned spins = 0;; ++spins) {
      const u64 g0 = __hip_atomic_load(slot + d0, RLX_AGENT), g1 = __hip_atomic_load(slot + d0 + 1, RLX_AGENT);
      const u64 gm = __hip_atomic_load(slot + D, RLX_AGENT), gl = __hip_atomic_load(slot + D + 1, RLX_AGENT);
      a0 = __uint_as_float((unsigned)g0); a1 = __uint_as_float((unsigned)g1);
      M = __uint_as_float((unsigned)gm); L = __uint_as_float((unsigned)gl);
      const bool ok = (unsigned)(g0 >> 32) == tag && (unsigned)(g1 >> 32) == tag && (unsigned)(gm >> 32) == tag && (unsigned)(gl >> 32) == tag;
      if (__all(ok)) break;
      if (spins > (1u << 22)) { L = __builtin_nanf(""); break; }
      __builtin_amdgcn_s_sleep(16);
    }
    if (lane == 0) __hip_atomic_store(epoch, tag, RLX_AGENT);
  }
  float* rec = lds + wave * (D + 4);
  *reinterpret_cast<f32x2*>(rec + 4 + d0) = f32x2{a0, a1};
  if (lane == 0) *reinterpret_cast<f32x2*>(rec) = f32x2{M, L};
  __syncthreads();
  if (tid < 64) {
    float mw[NW], lw[NW], MM = -1e30f;
#pragma unroll
    for (int w = 0; w < NW; ++w) {
      const f32x2 ml = *reinterpret_cast<const f32x2*>(lds + w * (D + 4));
      mw[w] = ml[0]; lw[w] = ml[1];
      MM = fmaxf(MM, mw[w]);
    }
    float LL = 0.f, o0 = 0.f, o1 = 0.f;
#pragma unroll
    for (int w = 0; w < NW; ++w) {
      const float s = __builtin_amdgcn_exp2f(mw[w] - MM);
      const f32x2 a = *reinterpret_cast<const f32x2*>(lds + w * (D + 4) + 4 + 2 * tid);
      LL = fmaf(s, lw[w], LL); o0 = fmaf(s, a[0], o0); o1 = fmaf(s, a[1], o1);
    }
    const float inv = 1.f / LL;
    const __hip_bfloat16 r0 = __float2bfloat16(o0 * inv), r1 = __float2bfloat16(o1 * inv);
    const unsigned pk = (unsigned)__builtin_bit_cast(unsigned short, r0) | ((unsigned)__builtin_bit_cast(unsigned short, r1) << 16);
    *reinterpret_cast<unsigned*>(out + (size_t)bh * D + 2 * tid) = pk;
  }
}
}

extern "C" void kernel_launch(void* const* d_in, const int* in_sizes, int n_in, void* d_out, int out_size,
                              void* d_ws, size_t ws_size, hipStream_t stream) {
  const unsigned kv_bytes = (unsigned)in_sizes[1] * 2u;
  paged_attn_kernel<<<B * H, NW * 64, 0, stream>>>(
      (const unsigned short*)d_in[0], (const unsigned short*)d_in[1], (const unsigned short*)d_in[2],
      (const int*)d_in[3], (const int*)d_in[4], (unsigned short*)d_out, (unsigned char*)d_ws, kv_bytes);
}
```

```cpp
#include <hip/hip_runtime.h>
#include <hip/hip_bf16.h>
#include <stdint.h>

#define AUX 2
#define HELP 1
#define HX 2
#define NS 3
#define DELTA 0
#define TIGHTK 1
#define PROBE 0
#define CPF 1
#define FORCEG 0
#define LSLP 8
#define LIN 1
#define CW 0
#define SLP 16
#define FMAXN 28
#define PWAIT 4

namespace {
constexpr int NW = NS + 1;
constexpr int B = 8, H = 32, D = 128, PAGE = 16, NBPS = 128, SKV = NBPS * PAGE;
constexpr unsigned TOK_BYTES = H * D * 2;
constexpr unsigned PAGE_BYTES = PAGE * TOK_BYTES;
constexpr unsigned OOB = 0x80000000u;
constexpr int SLOT_G = 256;
constexpr size_t WS_EPOCH = (size_t)B * H * SLOT_G * 8;
constexpr int REC = D + 4;
constexpr int GMAX = NBPS * FMAXN / (32 * NS);
constexpr int L_REC = 0, L_CTR = (NW + 1) * REC, L_FLAG = L_CTR + 1, L_PLAN = L_CTR + 4, L_FTAB = L_PLAN + 8, L_QF = L_FTAB + NBPS, L_END = L_QF + 64;

using bf16x2 = __attribute__((ext_vector_type(2))) __bf16;
using u32x4 = __attribute__((ext_vector_type(4))) unsigned;
using i32x4 = __attribute__((ext_vector_type(4))) int;
using f32x2 = __attribute__((ext_vector_type(2))) float;
typedef unsigned long long u64;
#define RLX_AGENT __ATOMIC_RELAXED, __HIP_MEMORY_SCOPE_AGENT
#define RLX_WG __ATOMIC_RELAXED, __HIP_MEMORY_SCOPE_WORKGROUP

__device__ __forceinline__ float dot2(unsigned k, unsigned q, float acc) {
  return __builtin_amdgcn_fdot2_f32_bf16(__builtin_bit_cast(bf16x2, k), __builtin_bit_cast(bf16x2, q), acc, false);
}
template <int CTRL> __device__ __forceinline__ float dpp(float x) {
  return __builtin_bit_cast(float, __builtin_amdgcn_mov_dpp(__builtin_bit_cast(int, x), CTRL, 0xf, 0xf, true));
}
__device__ __forceinline__ float row_sum(float x) {
  x += dpp<0x128>(x);
  x += dpp<0x124>(x);
  x += dpp<0x122>(x);
  x += dpp<0x121>(x);
  return x;
}
__device__ __forceinline__ float xrow_max(float x) {
  auto s = __builtin_amdgcn_permlane16_swap(__float_as_uint(x), __float_as_uint(x), false, false);
  x = fmaxf(__uint_as_float(s[0]), __uint_as_float(s[1]));
  auto t = __builtin_amdgcn_permlane32_swap(__float_as_uint(x), __float_as_uint(x), false, false);
  return fmaxf(__uint_as_float(t[0]), __uint_as_float(t[1]));
}
__device__ __forceinline__ float xrow_sum(float x) {
  auto s = __builtin_amdgcn_permlane16_swap(__float_as_uint(x), __float_as_uint(x), false, false);
  x = __uint_as_float(s[0]) + __uint_as_float(s[1]);
  auto t = __builtin_amdgcn_permlane32_swap(__float_as_uint(x), __float_as_uint(x), false, false);
  return __uint_as_float(t[0]) + __uint_as_float(t[1]);
}
__device__ __forceinline__ u64 granule(unsigned tag, float v) { return ((u64)tag << 32) | (u64)__float_as_uint(v); }
__device__ __forceinline__ int rfl(int x) { return __builtin_amdgcn_readfirstlane(x); }
#define SB() __builtin_amdgcn_sched_barrier(0)

__global__ void __launch_bounds__(NW * 64)
paged_attn_kernel(const unsigned short* __restrict__ q, const unsigned short* __restrict__ kc,
                  const unsigned short* __restrict__ vc, const int* __restrict__ bt,
                  const int* __restrict__ lens, unsigned short* __restrict__ out, unsigned char* ws, unsigned long long kv_bytes64) {
  __shared__ __attribute__((aligned(16))) int lds[L_END];
  unsigned cpf = 0;
  if (CPF) {
    u64 pc; unsigned off = (threadIdx.x & 63u) * 128u;
    asm volatile("s_getpc_b64 %0\n.Lcpf_here:\n\tv_min_u32 %1, .Lcpf_end-.Lcpf_here, %1" : "=s"(pc), "+v"(off));
    asm volatile("global_load_dword %0, %1, %2" : "=v"(cpf) : "v"(off), "s"(pc) : "memory");
  }
  const int bh = blockIdx.x, b = bh >> 5, h = bh & 31;
  const int tid = threadIdx.x, lane = tid & 63, r = lane >> 4, c = lane & 15;
  const int wave = rfl(tid >> 6);
  constexpr float C2 = 0.088388347648318440f * 1.4426950408889634f;

  unsigned kv_bytes = (unsigned)kv_bytes64;
  asm volatile("" : "+s"(kv_bytes) : "s"(out), "s"(ws));
  int len = lens[b];
  int phys0 = bt[b * NBPS + wave];
  SB();
  int bt0 = bt[b * NBPS + lane], bt1 = bt[b * NBPS + 64 + lane];
  u32x4 qown = *reinterpret_cast<const u32x4*>(q + (size_t)bh * D + 8 * c);
  int lv = lens[lane & (B - 1)];
  if (tid == 0) { lds[L_CTR] = NS; lds[L_FLAG] = 0; }
  const __amdgpu_buffer_rsrc_t krs = __builtin_amdgcn_make_buffer_rsrc((void*)kc, 0, kv_bytes, 0x00020000);
  const __amdgpu_buffer_rsrc_t vrs = __builtin_amdgcn_make_buffer_rsrc((void*)vc, 0, kv_bytes, 0x00020000);
  const unsigned lbase = (unsigned)r * TOK_BYTES + (unsigned)c * 16u;
  const bool allm = len <= 0;
  len = allm ? SKV : min(len, SKV);
  u32x4 Kb[4], Vb[4];
  unsigned vo[4];
#define OFFS(rem_) do { _Pragma("unroll") for (int t_ = 0; t_ < 4; ++t_) vo[t_] = (r + 4 * t_ < (rem_)) ? lbase + t_ * 4 * TOK_BYTES : OOB; } while (0)
#define ISSUE(X, rs, so_) do { _Pragma("unroll") for (int t_ = 0; t_ < 4; ++t_) X[t_] = __builtin_amdgcn_raw_buffer_load_b128(rs, vo[t_], so_, AUX); } while (0)
  int rem = wave < NS ? len - wave * PAGE : 0;
  unsigned so = (unsigned)phys0 * PAGE_BYTES + (unsigned)h * (D * 2);
  OFFS(rem);
  ISSUE(Kb, krs, so); SB();
  ISSUE(Vb, vrs, so); SB();
  asm volatile("s_waitcnt lgkmcnt(0)\n\ts_barrier" ::: "memory");

  const int np = (len + PAGE - 1) / PAGE;
  const int d0 = 8 * c + 2 * r;
  int budget = 1 << 20;
  int give = 0;
  unsigned rtag = 0;
  if (wave == NS) {
    const int sj = lane & (B - 1);
    rtag = __hip_atomic_load(reinterpret_cast<unsigned*>(ws + WS_EPOCH) + bh * 32, RLX_AGENT);
    const u64 allmask = __ballot(lv <= 0);
    lv = lv <= 0 ? SKV : min(lv, SKV);
    const int npv = (lv + PAGE - 1) / PAGE;
    const int key = lv * B + (B - 1 - sj);
    int rank = 0;
#pragma unroll
    for (int j = 0; j < B; ++j) rank += __builtin_amdgcn_readlane(key, j) > key ? 1 : 0;
    int tot = npv + __builtin_amdgcn_mov_dpp(npv, 0x124, 0xf, 0xf, true);
    tot += __builtin_amdgcn_mov_dpp(tot, 0x122, 0xf, 0xf, true);
    tot += __builtin_amdgcn_mov_dpp(tot, 0x121, 0xf, 0xf, true);
    const int pos = rank < B / 2 ? B - 2 - 2 * rank : 2 * rank - B + 1;
    const int mypos = __builtin_amdgcn_readlane(pos, b);
    const int cls = ((int)(reinterpret_cast<uintptr_t>(kc) >> 8) + (h ^ (((pos ^ mypos) & 1) * HX))) & 3;
    const int ex = npv - (tot / B + (pos < tot % B ? 1 : 0) + (cls == 1 ? -CW : cls == 3 ? CW : 0));
    const int pk = (ex << 3) | pos;
    int pre = 0;
#pragma unroll
    for (int j = 0; j < B; ++j) { const int pj = __builtin_amdgcn_readlane(pk, j); pre += (pj & 7) <= pos ? pj >> 3 : 0; }
    int pmin = min(pre, __builtin_amdgcn_mov_dpp(pre, 0x124, 0xf, 0xf, true));
    pmin = min(pmin, __builtin_amdgcn_mov_dpp(pmin, 0x122, 0xf, 0xf, true));
    pmin = min(pmin, __builtin_amdgcn_mov_dpp(pmin, 0x121, 0xf, 0xf, true));
    const int gv = HELP ? max(0, min(min(pre - pmin + FORCEG, tot * FMAXN / (32 * B * NS)), npv - 2 * NS)) : 0;
    const int pb = __builtin_ctzll(__ballot(pos == ((mypos + B - 1) & (B - 1))));
    const int dbh = pb * H + (h ^ HX);
    const int f0 = bt[pb * NBPS + lane], f1 = bt[pb * NBPS + 64 + lane];
    int qf = reinterpret_cast<const int*>(q)[dbh * (D / 2) + lane];
    const unsigned ftag = __hip_atomic_load(reinterpret_cast<unsigned*>(ws + WS_EPOCH) + dbh * 32, RLX_AGENT);
    give = __builtin_amdgcn_readlane(gv, b);
    const int F = __builtin_amdgcn_readlane(gv, pb);
    budget = np - give + F - (rfl(tot) / B + CW - DELTA);
    if ((allmask >> pb) & 1) qf = 0;
    lds[L_FTAB + lane] = f0; lds[L_FTAB + 64 + lane] = f1; lds[L_QF + lane] = qf;
    if (lane == 0) {
      *reinterpret_cast<i32x4*>(lds + L_PLAN) = i32x4{np - give, F, __builtin_amdgcn_readlane(lv, pb), dbh};
      lds[L_PLAN + 4] = (int)(ftag + 1u);
    }
    asm volatile("s_waitcnt lgkmcnt(0)" ::: "memory");
    if (lane == 0) __hip_atomic_store(lds + L_FLAG, 1, RLX_WG);
    __builtin_amdgcn_s_waitcnt(0x0F70);
  }

  float M = -1e30f, L = 0.f, a0 = 0.f, a1 = 0.f;
  if (budget > 0) {
    if (allm) qown = u32x4{0u, 0u, 0u, 0u};
    u32x4 qv = qown;
    float m = -1e30f, l = 0.f, acc[8];
    float sm = -1e30f, sl = 0.f, sacc[8];
#pragma unroll
    for (int k = 0; k < 8; ++k) { acc[k] = 0.f; sacc[k] = 0.f; }
    int own_np = max(min(np, 2 * NS), np - GMAX), fleft = 0, plen = 0, pnp = 0, dbh = 0, f0 = 0, f1 = 0;
    bool curf = false, planned = false;
    for (;;) {
      const bool nf = fleft > 0;
      const bool want = !nf && budget > 0;
      int cl = 1 << 20;
      if (want && lane == 0) cl = __hip_atomic_fetch_add(lds + L_CTR, 1, RLX_WG);
      const int pn = nf ? pnp - fleft : rfl(cl);
      fleft -= nf ? 1 : 0;
      if (!planned) {
        int fl;
        while ((fl = rfl(__hip_atomic_load(lds + L_FLAG, RLX_WG))) == 0 && pn >= own_np) __builtin_amdgcn_s_sleep(1);
        asm volatile("" ::: "memory");
        if (fl) {
          const i32x4 pl = *reinterpret_cast<const i32x4*>(lds + L_PLAN);
          own_np = rfl(pl[0]); plen = rfl(pl[2]); dbh = rfl(pl[3]);
          pnp = (plen + PAGE - 1) / PAGE;
          fleft = wave == NS - 1 ? rfl(pl[1]) : 0;
          f0 = lds[L_FTAB + lane]; f1 = lds[L_FTAB + 64 + lane];
          planned = true;
        }
      }
      const int remn = nf ? plen - pn * PAGE : pn < own_np ? len - pn * PAGE : 0;
      const int tb = (pn & 64) ? (nf ? f1 : bt1) : (nf ? f0 : bt0);
      so = (unsigned)__builtin_amdgcn_readlane(tb, pn & 63) * PAGE_BYTES + (unsigned)(nf ? h ^ HX : h) * (D * 2);
      budget -= want ? 1 : 0;
      OFFS(remn);
#pragma unroll
      for (int t = 0; t < 4; ++t) asm volatile("" : "+v"(vo[t]));
      asm volatile("" : "+s"(so));
      SB();
      float x[4];
#pragma unroll
      for (int t = 0; t < 4; ++t) {
        float d = 0.f;
#pragma unroll
        for (int k = 0; k < 4; ++k) d = dot2(Kb[t][k], qv[k], d);
        x[t] = d;
        if (TIGHTK) {
          asm volatile("" : "+v"(x[t]) :: "memory");
          SB();
          Kb[t] = __builtin_amdgcn_raw_buffer_load_b128(krs, vo[t], so, AUX);
          asm volatile("" ::: "memory");
          SB();
        }
      }
      if (!TIGHTK) {
#pragma unroll
        for (int t = 0; t < 4; ++t) asm volatile("" : "+v"(x[t]) :: "memory");
        SB();
        ISSUE(Kb, krs, so);
        asm volatile("" ::: "memory");
        SB();
      }
      float mn = m;
#pragma unroll
      for (int t = 0; t < 4; ++t) {
        x[t] = row_sum(x[t]) * C2;
        x[t] = (r + 4 * t < rem) ? x[t] : -INFINITY;
        mn = fmaxf(mn, x[t]);
      }
      const float alpha = __builtin_amdgcn_exp2f(m - mn);
      m = mn;
      float p[4], ps = 0.f;
#pragma unroll
      for (int t = 0; t < 4; ++t) { p[t] = __builtin_amdgcn_exp2f(x[t] - mn); ps += p[t]; }
      l = fmaf(l, alpha, ps);
#pragma unroll
      for (int k = 0; k < 8; ++k) acc[k] *= alpha;
      SB();
#pragma unroll
      for (int t = 0; t < 4; ++t) {
#pragma unroll
        for (int k = 0; k < 4; ++k) {
          const unsigned w = Vb[t][k];
          acc[2 * k] = fmaf(p[t], __builtin_bit_cast(float, w << 16), acc[2 * k]);
          acc[2 * k + 1] = fmaf(p[t], __builtin_bit_cast(float, w & 0xffff0000u), acc[2 * k + 1]);
        }
      }
      asm volatile("" : "+v"(acc[0]), "+v"(acc[1]), "+v"(acc[2]), "+v"(acc[3]), "+v"(acc[4]), "+v"(acc[5]), "+v"(acc[6]), "+v"(acc[7]) :: "memory");
      SB();
      ISSUE(Vb, vrs, so);
      asm volatile("" ::: "memory");
      SB();
      const bool pub = curf && !nf;
      const bool fin = !curf && remn <= 0 && fleft == 0;
      if (pub || fin) {
        M = xrow_max(m);
        const float sc = __builtin_amdgcn_exp2f(m - M);
        L = xrow_sum(l * sc);
#pragma unroll
        for (int k = 0; k < 8; ++k) acc[k] = xrow_sum(acc[k] * sc);
        a0 = r == 0 ? acc[0] : r == 1 ? acc[2] : r == 2 ? acc[4] : acc[6];
        a1 = r == 0 ? acc[1] : r == 1 ? acc[3] : r == 2 ? acc[5] : acc[7];
        if (fin) break;
        const unsigned tag = (unsigned)lds[L_PLAN + 4];
        u64* slot = reinterpret_cast<u64*>(ws) + (size_t)dbh * SLOT_G;
        __hip_atomic_store(slot + (LIN ? lane : d0), granule(tag, a0), RLX_AGENT);
        __hip_atomic_store(slot + (LIN ? 64 + lane : d0 + 1), granule(tag, a1), RLX_AGENT);
        if (lane < (LIN ? 16 : 2)) __hip_atomic_store(slot + D + lane, granule(tag, lane ? L : M), RLX_AGENT);
        m = sm; l = sl;
#pragma unroll
        for (int k = 0; k < 8; ++k) acc[k] = sacc[k];
        qv = qown;
      }
      if (!curf && nf) {
        sm = m; sl = l; m = -1e30f; l = 0.f;
#pragma unroll
        for (int k = 0; k < 8; ++k) { sacc[k] = acc[k]; acc[k] = 0.f; }
        qv = *reinterpret_cast<const u32x4*>(lds + L_QF + 4 * c);
      }
      curf = nf; rem = remn;
    }
  }
  float* ldsf = reinterpret_cast<float*>(lds);
  if (wave == NS) {
    float fM = -1e30f, fL = 0.f, fa0 = 0.f, fa1 = 0.f;
    if (give > 0) {
      const unsigned tag = rtag + 1u;
      const u64* slot = reinterpret_cast<const u64*>(ws) + (size_t)bh * SLOT_G;
      const int thr = min(NS * (give + PWAIT), np - give);
      while (__hip_atomic_load(lds + L_CTR, RLX_WG) < thr) __builtin_amdgcn_s_sleep(LSLP);
      for (unsigned spins = 0;; ++spins) {
        const u64 g0 = __hip_atomic_load(slot + (LIN ? lane : d0), RLX_AGENT), g1 = __hip_atomic_load(slot + (LIN ? 64 + lane : d0 + 1), RLX_AGENT);
        const u64 gm = __hip_atomic_load(slot + D, RLX_AGENT), gl = __hip_atomic_load(slot + D + 1, RLX_AGENT);
        fa0 = __uint_as_float((unsigned)g0); fa1 = __uint_as_float((unsigned)g1);
        fM = __uint_as_float((unsigned)gm); fL = __uint_as_float((unsigned)gl);
        const bool ok = (unsigned)(g0 >> 32) == tag && (unsigned)(g1 >> 32) == tag && (unsigned)(gm >> 32) == tag && (unsigned)(gl >> 32) == tag;
        if (__all(ok)) break;
        if (spins > (1u << 22)) { fL = __builtin_nanf(""); break; }
        __builtin_amdgcn_s_sleep(SLP);
      }
      if (lane == 0) __hip_atomic_store(reinterpret_cast<unsigned*>(ws + WS_EPOCH) + bh * 32, tag, RLX_AGENT);
    }
    float* frec = ldsf + NW * REC;
    *reinterpret_cast<f32x2*>(frec + 4 + d0) = f32x2{fa0, fa1};
    if (lane == 0) *reinterpret_cast<f32x2*>(frec) = f32x2{fM, fL};
  }
  float* rec = ldsf + wave * REC;
  *reinterpret_cast<f32x2*>(rec + 4 + d0) = f32x2{a0, a1};
  if (lane == 0) *reinterpret_cast<f32x2*>(rec) = f32x2{M, L};
  if (CPF) asm volatile("" :: "v"(cpf));
  __syncthreads();
  if (tid < 64) {
    float mw[NW + 1], lw[NW + 1], MM = -1e30f;
#pragma unroll
    for (int w = 0; w <= NW; ++w) {
      const f32x2 ml = *reinterpret_cast<const f32x2*>(ldsf + w * REC);
      mw[w] = ml[0]; lw[w] = ml[1];
      MM = fmaxf(MM, mw[w]);
    }
    float LL = 0.f, o0 = 0.f, o1 = 0.f;
#pragma unroll
    for (int w = 0; w <= NW; ++w) {
      const float s = __builtin_amdgcn_exp2f(mw[w] - MM);
      const f32x2 a = *reinterpret_cast<const f32x2*>(ldsf + w * REC + 4 + 2 * tid);
      LL = fmaf(s, lw[w], LL); o0 = fmaf(s, a[0], o0); o1 = fmaf(s, a[1], o1);
    }
    const float inv = 1.f / LL;
    const __hip_bfloat16 r0 = __float2bfloat16(o0 * inv), r1 = __float2bfloat16(o1 * inv);
    const unsigned pk = (unsigned)__builtin_bit_cast(unsigned short, r0) | ((unsigned)__builtin_bit_cast(unsigned short, r1) << 16);
    if (CPF) asm volatile(".Lcpf_end:");
    *reinterpret_cast<unsigned*>(out + (size_t)bh * D + 2 * tid) = pk;
  }
}
}

extern "C" void kernel_launch(void* const* d_in, const int* in_sizes, int n_in, void* d_out, int out_size,
                              void* d_ws, size_t ws_size, hipStream_t stream) {
  const unsigned long long kv_bytes = (unsigned long long)in_sizes[1] * 2ull;
  paged_attn_kernel<<<B * H, NW * 64, 0, stream>>>(
      (const unsigned short*)d_in[0], (const unsigned short*)d_in[1], (const unsigned short*)d_in[2],
      (const int*)d_in[3], (const int*)d_in[4], (unsigned short*)d_out, (unsigned char*)d_ws, kv_bytes);
}
```

```cpp
#include <hip/hip_runtime.h>
#include <hip/hip_bf16.h>
#include <stdint.h>

#define AUX 2
#define HELP 1
#define HX 2
#define DELTA 1
#define TIGHTK 1
#define CPF 1
#define PROBE 0
#define RCP 1
#define PRO 1
#define FSPLIT 12
#define WARM 1
#define COMPACT 1
#define QNT 1
#define OUTW 0
#define SLP 6
#define FMAXN 48
#define K0 2
#define PWAIT 6
#define TLBW 0

namespace {
constexpr int NS = 3, NW = NS + 1;
constexpr int B = 8, H = 32, D = 128, PAGE = 16, NBPS = 128, SKV = NBPS * PAGE;
constexpr unsigned TOK_BYTES = H * D * 2;
constexpr unsigned PAGE_BYTES = PAGE * TOK_BYTES;
constexpr unsigned OOB = 0x80000000u;
constexpr unsigned SLOT_BYTES = 1024, SLOT_FLAG = COMPACT ? 544 : 640, SLOT_EPOCH = COMPACT ? 576 : 768;
constexpr int REC = D + 4;
constexpr int L_PROG = (NW + 3) * REC, L_FLAG = L_PROG + 1, L_SEND = L_PROG + 2, L_RNG = L_PROG + 4, L_FP = L_RNG + 4 * NS, L_FTAB = L_FP + 4, L_QF = L_FTAB + NBPS, L_END = L_QF + 64;

using bf16x2 = __attribute__((ext_vector_type(2))) __bf16;
using u32x4 = __attribute__((ext_vector_type(4))) unsigned;
using i32x4 = __attribute__((ext_vector_type(4))) int;
using i32x2 = __attribute__((ext_vector_type(2))) int;
using f32x2 = __attribute__((ext_vector_type(2))) float;
using f32x4 = __attribute__((ext_vector_type(4))) float;
typedef unsigned long long u64;
#define RLX_AGENT __ATOMIC_RELAXED, __HIP_MEMORY_SCOPE_AGENT
#define RLX_WG __ATOMIC_RELAXED, __HIP_MEMORY_SCOPE_WORKGROUP

__device__ __forceinline__ float dot2(unsigned k, unsigned q, float acc) {
  return __builtin_amdgcn_fdot2_f32_bf16(__builtin_bit_cast(bf16x2, k), __builtin_bit_cast(bf16x2, q), acc, false);
}
template <int CTRL> __device__ __forceinline__ float dpp(float x) {
  return __builtin_bit_cast(float, __builtin_amdgcn_mov_dpp(__builtin_bit_cast(int, x), CTRL, 0xf, 0xf, true));
}
template <int CTRL> __device__ __forceinline__ int dppi(int x) { return __builtin_amdgcn_mov_dpp(x, CTRL, 0xf, 0xf, true); }
__device__ __forceinline__ float row_sum(float x) {
  x += dpp<0x128>(x);
  x += dpp<0x124>(x);
  x += dpp<0x122>(x);
  x += dpp<0x121>(x);
  return x;
}
__device__ __forceinline__ float xrow_max(float x) {
  auto s = __builtin_amdgcn_permlane16_swap(__float_as_uint(x), __float_as_uint(x), false, false);
  x = fmaxf(__uint_as_float(s[0]), __uint_as_float(s[1]));
  auto t = __builtin_amdgcn_permlane32_swap(__float_as_uint(x), __float_as_uint(x), false, false);
  return fmaxf(__uint_as_float(t[0]), __uint_as_float(t[1]));
}
__device__ __forceinline__ float xrow_sum(float x) {
  auto s = __builtin_amdgcn_permlane16_swap(__float_as_uint(x), __float_as_uint(x), false, false);
  x = __uint_as_float(s[0]) + __uint_as_float(s[1]);
  auto t = __builtin_amdgcn_permlane32_swap(__float_as_uint(x), __float_as_uint(x), false, false);
  return __uint_as_float(t[0]) + __uint_as_float(t[1]);
}
__device__ __forceinline__ int rfl(int x) { return __builtin_amdgcn_readfirstlane(x); }
#define SB() __builtin_amdgcn_sched_barrier(0)

__global__ void __launch_bounds__(NW * 64, 1)
paged_attn_kernel(const unsigned short* __restrict__ q, const unsigned short* __restrict__ kc,
                  const unsigned short* __restrict__ vc, const int* __restrict__ bt,
                  const int* __restrict__ lens, unsigned short* __restrict__ out, unsigned char* ws, unsigned long long kv_bytes64) {
  __shared__ __attribute__((aligned(16))) int lds[L_END];
  unsigned cpf = 0;
  if (CPF) {
    u64 pc; unsigned off = (threadIdx.x & 63u) * 128u;
    asm volatile("s_getpc_b64 %0\n.Lcpf_here:\n\tv_min_u32 %1, .Lcpf_end-.Lcpf_here, %1" : "=s"(pc), "+v"(off));
    cpf = *reinterpret_cast<const __attribute__((address_space(1))) unsigned*>(pc + off);
    asm volatile("" ::: "memory");
  }
  const int bh = blockIdx.x, b = bh >> 5, h = bh & 31;
  const int tid = threadIdx.x, lane = tid & 63, r = lane >> 4, c = lane & 15;
  const int wave = rfl(tid >> 6);
  constexpr float C2 = 0.088388347648318440f * 1.4426950408889634f;

  unsigned kv_bytes = (unsigned)kv_bytes64;
  asm volatile("" : "+s"(kv_bytes) : "s"(out), "s"(ws));
  int len = lens[b];
  int phys0 = bt[b * NBPS + wave];
  SB();
  unsigned wsl = 0;
  unsigned wso = 0;
  if (WARM == 1) wsl = __hip_atomic_load(reinterpret_cast<const unsigned*>(ws + (size_t)bh * SLOT_BYTES + min(lane, COMPACT ? 9 : 15) * 64), RLX_AGENT);
  if (WARM == 2) wsl = *reinterpret_cast<const unsigned*>(ws + (size_t)bh * SLOT_BYTES + min(lane, COMPACT ? 9 : 15) * 64);
  if (OUTW) wso = __hip_atomic_load(reinterpret_cast<const unsigned*>(out + (size_t)bh * D) + lane, RLX_AGENT);
  int bt0 = bt[b * NBPS + lane], bt1 = bt[b * NBPS + 64 + lane];
  u32x4 qown = QNT ? __builtin_nontemporal_load(reinterpret_cast<const u32x4*>(q + (size_t)bh * D + 8 * c)) : *reinterpret_cast<const u32x4*>(q + (size_t)bh * D + 8 * c);
  int lv = lens[lane & (B - 1)];
  if (tid == 0) *reinterpret_cast<i32x4*>(lds + L_PROG) = i32x4{0, 0, 0, 0};
  const __amdgpu_buffer_rsrc_t krs = __builtin_amdgcn_make_buffer_rsrc((void*)kc, 0, kv_bytes, 0x00020000);
  const __amdgpu_buffer_rsrc_t vrs = __builtin_amdgcn_make_buffer_rsrc((void*)vc, 0, kv_bytes, 0x00020000);
  const unsigned lbase = (unsigned)r * TOK_BYTES + (unsigned)c * 16u;
  unsigned vb[4]; int rt[4];
#pragma unroll
  for (int t = 0; t < 4; ++t) { vb[t] = lbase + t * 4 * TOK_BYTES; rt[t] = r + 4 * t; if (PRO) asm volatile("" : "+v"(vb[t]), "+v"(rt[t])); }
  if (PRO) SB();
  const bool allm = len <= 0;
  len = allm ? SKV : min(len, SKV);
  u32x4 Kb[4], Vb[4];
  unsigned vo[4];
#define OFFS(rem_) do { _Pragma("unroll") for (int t_ = 0; t_ < 4; ++t_) vo[t_] = (rt[t_] < (rem_)) ? vb[t_] : OOB; } while (0)
#define ISSUE(X, rs, so_) do { _Pragma("unroll") for (int t_ = 0; t_ < 4; ++t_) X[t_] = __builtin_amdgcn_raw_buffer_load_b128(rs, vo[t_], so_, AUX); } while (0)
  int rem = wave < NS ? len - wave * PAGE : 0;
  unsigned so = (unsigned)phys0 * PAGE_BYTES + (unsigned)h * (D * 2);
  OFFS(rem);
  ISSUE(Kb, krs, so); SB();
  ISSUE(Vb, vrs, so); SB();
  asm volatile("s_waitcnt lgkmcnt(0)\n\ts_barrier" ::: "memory");

  const int np = (len + PAGE - 1) / PAGE;
  const int d0 = 8 * c + 2 * r;
  int pnext = 0, pend = 0;
  int give = 0;
  int F = 0, dbh = 0, f1n = 0, g1n = 0;
  u64 rtag = 0, ftag = 0;
  if (wave == NS) {
    const int sj = lane & (B - 1);
    rtag = __hip_atomic_load(reinterpret_cast<u64*>(ws + (size_t)bh * SLOT_BYTES + SLOT_EPOCH), RLX_AGENT);
    const u64 allmask = __ballot(lv <= 0);
    lv = lv <= 0 ? SKV : min(lv, SKV);
    const int npv = (lv + PAGE - 1) / PAGE;
    const int key = lv * B + (B - 1 - sj);
    int rank = 0;
#pragma unroll
    for (int j = 0; j < B; ++j) rank += __builtin_amdgcn_readlane(key, j) > key ? 1 : 0;
    int tot = npv + dppi<0x124>(npv);
    tot += dppi<0x122>(tot);
    tot += dppi<0x121>(tot);
    const int pos = rank < B / 2 ? B - 2 - 2 * rank : 2 * rank - B + 1;
    const int mypos = __builtin_amdgcn_readlane(pos, b);
    const int pb = __builtin_ctzll(__ballot(pos == ((mypos + B - 1) & (B - 1))));
    dbh = pb * H + (h ^ HX);
    const int f0 = bt[pb * NBPS + lane], f1 = bt[pb * NBPS + 64 + lane];
    int qf = QNT ? __builtin_nontemporal_load(reinterpret_cast<const int*>(q) + dbh * (D / 2) + lane) : reinterpret_cast<const int*>(q)[dbh * (D / 2) + lane];
    ftag = __hip_atomic_load(reinterpret_cast<u64*>(ws + (size_t)dbh * SLOT_BYTES + SLOT_EPOCH), RLX_AGENT);
    const int ex = npv - (tot / B + (pos < tot % B ? 1 : 0));
    const int pk = (ex << 3) | pos;
    int pre = 0;
#pragma unroll
    for (int j = 0; j < B; ++j) { const int pj = __builtin_amdgcn_readlane(pk, j); pre += (pj & 7) <= pos ? pj >> 3 : 0; }
    int pmin = min(pre, dppi<0x124>(pre));
    pmin = min(pmin, dppi<0x122>(pmin));
    pmin = min(pmin, dppi<0x121>(pmin));
    const int gv = HELP ? max(0, min(min(pre - pmin, tot * FMAXN / (32 * B * NS)), npv - K0 * NS)) : 0;
    give = __builtin_amdgcn_readlane(gv, b);
    F = __builtin_amdgcn_readlane(gv, pb);
    const int lo = min(np, K0 * NS), own = np - give - lo;
    const int top = max(0, min(own + F + lo - rfl(tot) / (B * NS) * NS, (own + F + lo) / NW));
    const int S = min(top, own), rest = own - S;
    const int pl = __builtin_amdgcn_readlane(lv, pb), pnp = (pl + PAGE - 1) / PAGE;
    f1n = F * (32 * B * NS) > rfl(tot) * FSPLIT ? F / 2 : 0;
    g1n = give * (32 * B * NS) > rfl(tot) * FSPLIT ? give / 2 : 0;
    const int c2 = max(0, min((rest + F) / NS - (F - f1n), rest));
    const int c1 = max(0, min((rest + F + 1) / NS - f1n, rest - c2)), c0 = rest - c1 - c2;
    if (lane == 0) {
      *reinterpret_cast<i32x4*>(lds + L_RNG) = i32x4{lo, lo + c0, 0, 0};
      *reinterpret_cast<i32x4*>(lds + L_RNG + 4) = i32x4{lo + c0, lo + c0 + c1, pnp - F, pnp - F + f1n};
      *reinterpret_cast<i32x4*>(lds + L_RNG + 8) = i32x4{lo + c0 + c1, lo + rest, pnp - F + f1n, pnp};
      lds[L_FP] = pl;
      asm volatile("s_waitcnt lgkmcnt(0)" ::: "memory");
      __hip_atomic_store(lds + L_FLAG, 1, RLX_WG);
    }
    pnext = lo + rest; pend = pnext + S;
    if ((allmask >> pb) & 1) qf = 0;
    lds[L_FTAB + lane] = f0; lds[L_FTAB + 64 + lane] = f1; lds[L_QF + lane] = qf;
    asm volatile("s_waitcnt lgkmcnt(0)" ::: "memory");
    if (lane == 0) __hip_atomic_store(lds + L_FLAG, 2, RLX_WG);
    __builtin_amdgcn_s_waitcnt(0x0F70);
  }

  float M = -1e30f, L = 0.f, a0 = 0.f, a1 = 0.f;
  if (wave < NS || pnext < pend) {
    if (allm) qown = u32x4{0u, 0u, 0u, 0u};
    u32x4 qv = qown;
    float m = -1e30f, l = 0.f, acc[8];
    float sm = -1e30f, sl = 0.f, sacc[8];
#pragma unroll
    for (int k = 0; k < 8; ++k) { acc[k] = 0.f; sacc[k] = 0.f; }
    int fnext = 0, fend = 0, plen = 0, f0 = 0, f1 = 0;
    bool curf = false;
    for (int it = wave < NS ? 1 : K0 + 1;; ++it) {
      if (it == K0) {
        while (rfl(__hip_atomic_load(lds + L_FLAG, RLX_WG)) < 1) __builtin_amdgcn_s_sleep(1);
        asm volatile("" ::: "memory");
        const i32x4 rg = *reinterpret_cast<const i32x4*>(lds + L_RNG + 4 * wave);
        pnext = rfl(rg[0]); pend = rfl(rg[1]); fnext = rfl(rg[2]); fend = rfl(rg[3]);
        if (fnext < fend) {
          plen = rfl(lds[L_FP]);
          while (rfl(__hip_atomic_load(lds + L_FLAG, RLX_WG)) < 2) __builtin_amdgcn_s_sleep(1);
          asm volatile("" ::: "memory");
          f0 = lds[L_FTAB + lane]; f1 = lds[L_FTAB + 64 + lane];
        }
      }
      if (tid == 0) __hip_atomic_store(lds + L_PROG, it, RLX_WG);
      const bool nf = fnext < fend, fixed = it < K0;
      const int pn = nf ? fnext : fixed ? it * NS + wave : pnext;
      const int remn = nf ? plen - pn * PAGE : (fixed || pn < pend) ? len - pn * PAGE : 0;
      const int tb = (pn & 64) ? (nf ? f1 : bt1) : (nf ? f0 : bt0);
      so = (unsigned)__builtin_amdgcn_readlane(tb, pn & 63) * PAGE_BYTES + (unsigned)(nf ? h ^ HX : h) * (D * 2);
      fnext += nf ? 1 : 0;
      pnext += (nf || fixed) ? 0 : 1;
      OFFS(remn);
#pragma unroll
      for (int t = 0; t < 4; ++t) asm volatile("" : "+v"(vo[t]));
      asm volatile("" : "+s"(so));
      SB();
      float x[4];
#pragma unroll
      for (int t = 0; t < 4; ++t) {
        float d = 0.f;
#pragma unroll
        for (int k = 0; k < 4; ++k) d = dot2(Kb[t][k], qv[k], d);
        x[t] = d;
        if (TIGHTK) {
          asm volatile("" : "+v"(x[t]) :: "memory");
          SB();
          Kb[t] = __builtin_amdgcn_raw_buffer_load_b128(krs, vo[t], so, AUX);
          asm volatile("" ::: "memory");
          SB();
        }
      }
      if (!TIGHTK) {
#pragma unroll
        for (int t = 0; t < 4; ++t) asm volatile("" : "+v"(x[t]) :: "memory");
        SB();
        ISSUE(Kb, krs, so);
        asm volatile("" ::: "memory");
        SB();
      }
      float mn = m;
#pragma unroll
      for (int t = 0; t < 4; ++t) {
        x[t] = row_sum(x[t]) * C2;
        x[t] = (rt[t] < rem) ? x[t] : -INFINITY;
        mn = fmaxf(mn, x[t]);
      }
      const float alpha = __builtin_amdgcn_exp2f(m - mn);
      m = mn;
      float p[4], ps = 0.f;
#pragma unroll
      for (int t = 0; t < 4; ++t) { p[t] = __builtin_amdgcn_exp2f(x[t] - mn); ps += p[t]; }
      l = fmaf(l, alpha, ps);
#pragma unroll
      for (int k = 0; k < 8; ++k) acc[k] *= alpha;
      SB();
#pragma unroll
      for (int t = 0; t < 4; ++t) {
#pragma unroll
        for (int k = 0; k < 4; ++k) {
          const unsigned w = Vb[t][k];
          acc[2 * k] = fmaf(p[t], __builtin_bit_cast(float, w << 16), acc[2 * k]);
          acc[2 * k + 1] = fmaf(p[t], __builtin_bit_cast(float, w & 0xffff0000u), acc[2 * k + 1]);
        }
      }
      asm volatile("" : "+v"(acc[0]), "+v"(acc[1]), "+v"(acc[2]), "+v"(acc[3]), "+v"(acc[4]), "+v"(acc[5]), "+v"(acc[6]), "+v"(acc[7]) :: "memory");
      SB();
      ISSUE(Vb, vrs, so);
      asm volatile("" ::: "memory");
      SB();
      const bool pub = curf && !nf;
      const bool fin = !curf && remn <= 0 && !fixed;
      if (pub || fin) {
        M = xrow_max(m);
        const float sc = __builtin_amdgcn_exp2f(m - M);
        L = xrow_sum(l * sc);
#pragma unroll
        for (int k = 0; k < 8; ++k) acc[k] = xrow_sum(acc[k] * sc);
        a0 = r == 0 ? acc[0] : r == 1 ? acc[2] : r == 2 ? acc[4] : acc[6];
        a1 = r == 0 ? acc[1] : r == 1 ? acc[3] : r == 2 ? acc[5] : acc[7];
        if (fin) break;
        float* srec = reinterpret_cast<float*>(lds) + (NW + wave) * REC;
        *reinterpret_cast<f32x2*>(srec + 4 + d0) = f32x2{a0, a1};
        if (lane == 0) *reinterpret_cast<f32x2*>(srec) = f32x2{M, L};
        asm volatile("s_waitcnt lgkmcnt(0)" ::: "memory");
        if (lane == 0) __hip_atomic_store(lds + L_SEND + wave - 1, 1, RLX_WG);
        m = sm; l = sl;
#pragma unroll
        for (int k = 0; k < 8; ++k) acc[k] = sacc[k];
        qv = qown;
      }
      if (!curf && nf) {
        sm = m; sl = l; m = -1e30f; l = 0.f;
#pragma unroll
        for (int k = 0; k < 8; ++k) { sacc[k] = acc[k]; acc[k] = 0.f; }
        qv = *reinterpret_cast<const u32x4*>(lds + L_QF + 4 * c);
      }
      curf = nf; rem = remn;
    }
    if (tid == 0) __hip_atomic_store(lds + L_PROG, 1 << 20, RLX_WG);
  }
  float* ldsf = reinterpret_cast<float*>(lds);
  if (wave == NS) {
    const __amdgpu_buffer_rsrc_t wrs = __builtin_amdgcn_make_buffer_rsrc(ws, 0, B * H * SLOT_BYTES, 0x00020000);
    const unsigned loff = lane < REC / 4 ? lane * 16u : OOB;
    u32x4* lrec = reinterpret_cast<u32x4*>(lds + NW * REC) + min(lane, REC / 4 - 1);
    if (give <= 0) *lrec = u32x4{lane ? 0u : __float_as_uint(-1e30f), 0u, 0u, 0u};
    bool send = F > 0, recv = give > 0;
    for (unsigned spins = 0; send || recv; ++spins) {
      const int prog = rfl(__hip_atomic_load(lds + L_PROG, RLX_WG));
      const int s1 = rfl(__hip_atomic_load(lds + L_SEND, RLX_WG)), s2 = rfl(__hip_atomic_load(lds + L_SEND + 1, RLX_WG));
      if (send && s2 && (s1 || f1n == 0)) {
        asm volatile("" ::: "memory");
        f32x4 rec2 = *reinterpret_cast<const f32x4*>(lrec + 2 * (REC / 4));
        if (f1n) {
          const f32x4 rec1 = *reinterpret_cast<const f32x4*>(lrec + REC / 4);
          const float m1 = ldsf[(NW + 1) * REC], m2 = ldsf[(NW + 2) * REC], mm = fmaxf(m1, m2);
          const float e1 = __builtin_amdgcn_exp2f(m1 - mm), e2 = __builtin_amdgcn_exp2f(m2 - mm);
          rec2 = rec1 * e1 + rec2 * e2;
          if (lane == 0) rec2[0] = mm;
        }
        __builtin_amdgcn_raw_buffer_store_b128(__builtin_bit_cast(u32x4, rec2), wrs, loff, (unsigned)rfl(dbh) * SLOT_BYTES, 16);
        asm volatile("s_waitcnt vmcnt(0)" ::: "memory");
        if (lane == 0) __hip_atomic_store(reinterpret_cast<u64*>(ws + (size_t)dbh * SLOT_BYTES + SLOT_FLAG), ftag + 1u, RLX_AGENT);
        send = false;
      }
      if (recv && prog >= give - g1n + PWAIT) {
        const u64 fl = __hip_atomic_load(reinterpret_cast<u64*>(ws + (size_t)bh * SLOT_BYTES + SLOT_FLAG), RLX_AGENT);
        if (__all(fl == rtag + 1u)) {
          asm volatile("" ::: "memory");
          const u32x4 pay = __builtin_amdgcn_raw_buffer_load_b128(wrs, loff, (unsigned)bh * SLOT_BYTES, 16);
          if (lane < REC / 4) *lrec = pay;
          asm volatile("s_waitcnt vmcnt(0)" ::: "memory");
          if (lane == 0) __hip_atomic_store(reinterpret_cast<u64*>(ws + (size_t)bh * SLOT_BYTES + SLOT_EPOCH), rtag + 1u, RLX_AGENT);
          recv = false;
        } else if (spins > (1u << 23)) {
          *lrec = u32x4{0u, 0x7fc00000u, 0u, 0u};
          recv = false;
        }
      }
      __builtin_amdgcn_s_sleep(SLP);
    }
  }
  float* rec = ldsf + wave * REC;
  *reinterpret_cast<f32x2*>(rec + 4 + d0) = f32x2{a0, a1};
  if (lane == 0) *reinterpret_cast<f32x2*>(rec) = f32x2{M, L};
  if (CPF) asm volatile("" :: "v"(cpf), "v"(wsl), "v"(wso));
  __syncthreads();
  if (tid < 64) {
    float mw[NW + 1], lw[NW + 1], MM = -1e30f;
#pragma unroll
    for (int w = 0; w <= NW; ++w) {
      const f32x2 ml = *reinterpret_cast<const f32x2*>(ldsf + w * REC);
      mw[w] = ml[0]; lw[w] = ml[1];
      MM = fmaxf(MM, mw[w]);
    }
    float LL = 0.f, o0 = 0.f, o1 = 0.f;
#pragma unroll
    for (int w = 0; w <= NW; ++w) {
      const float s = __builtin_amdgcn_exp2f(mw[w] - MM);
      const f32x2 a = *reinterpret_cast<const f32x2*>(ldsf + w * REC + 4 + 2 * tid);
      LL = fmaf(s, lw[w], LL); o0 = fmaf(s, a[0], o0); o1 = fmaf(s, a[1], o1);
    }
    const float inv = RCP ? __builtin_amdgcn_rcpf(LL) : 1.f / LL;
    const __hip_bfloat16 r0 = __float2bfloat16(o0 * inv), r1 = __float2bfloat16(o1 * inv);
    const unsigned pk = (unsigned)__builtin_bit_cast(unsigned short, r0) | ((unsigned)__builtin_bit_cast(unsigned short, r1) << 16);
    if (CPF) asm volatile(".Lcpf_end:");
    *reinterpret_cast<unsigned*>(out + (size_t)bh * D + 2 * tid) = pk;
  }
}
}

extern "C" void kernel_launch(void* const* d_in, const int* in_sizes, int n_in, void* d_out, int out_size,
                              void* d_ws, size_t ws_size, hipStream_t stream) {
  const unsigned long long kv_bytes = (unsigned long long)in_sizes[1] * 2ull;
  paged_attn_kernel<<<B * H, NW * 64, 0, stream>>>(
      (const unsigned short*)d_in[0], (const unsigned short*)d_in[1], (const unsigned short*)d_in[2],
      (const int*)d_in[3], (const int*)d_in[4], (unsigned short*)d_out, (unsigned char*)d_ws, kv_bytes);
}
```

```cpp
#include <hip/hip_runtime.h>
#include <hip/hip_bf16.h>
#include <stdint.h>

#define AUX 2
#define HELP 1
#define HX 2
#define DELTA 1
#define TIGHTK -1
#define CPF 1
#define PROBE 0
#define RCP 1
#define PRO 1
#define FSPLIT 12
#define WARM 1
#define COMPACT 1
#define QNT 1
#define OUTW 0
#define TIGHTV 0
#define SVLATE 1
#define PK 1
#define RND (B * NS * 2 - 1)
#define DRY 0
#define SLP 6
#define FMAXN 48
#define K0 4
#define PWAIT 6
#define TLBW 0

namespace {
constexpr int NS = 3, NW = NS + 1;
constexpr int B = 8, H = 32, D = 128, PAGE = 16, NBPS = 128, SKV = NBPS * PAGE;
constexpr unsigned TOK_BYTES = H * D * 2;
constexpr unsigned PAGE_BYTES = PAGE * TOK_BYTES;
constexpr unsigned OOB = 0x80000000u;
constexpr unsigned SLOT_BYTES = 1024, SLOT_FLAG = COMPACT ? 544 : 640, SLOT_EPOCH = COMPACT ? 576 : 768;
constexpr int REC = D + 4;
constexpr int L_PROG = (NW + 3) * REC, L_FLAG = L_PROG + 1, L_SEND = L_PROG + 2, L_RNG = L_PROG + 4, L_FP = L_RNG + 4 * NS, L_FTAB = L_FP + 4, L_QF = L_FTAB + NBPS, L_SC = L_QF + 64, L_END = L_SC + NW * 64 * PAGE;

using bf16x2 = __attribute__((ext_vector_type(2))) __bf16;
using u32x4 = __attribute__((ext_vector_type(4))) unsigned;
using i32x4 = __attribute__((ext_vector_type(4))) int;
using i32x2 = __attribute__((ext_vector_type(2))) int;
using f32x2 = __attribute__((ext_vector_type(2))) float;
using f32x4 = __attribute__((ext_vector_type(4))) float;
typedef unsigned long long u64;
#define RLX_AGENT __ATOMIC_RELAXED, __HIP_MEMORY_SCOPE_AGENT
#define RLX_WG __ATOMIC_RELAXED, __HIP_MEMORY_SCOPE_WORKGROUP

__device__ __forceinline__ float dot2(unsigned k, unsigned q, float acc) {
  return __builtin_amdgcn_fdot2_f32_bf16(__builtin_bit_cast(bf16x2, k), __builtin_bit_cast(bf16x2, q), acc, false);
}
template <int CTRL> __device__ __forceinline__ float dpp(float x) {
  return __builtin_bit_cast(float, __builtin_amdgcn_mov_dpp(__builtin_bit_cast(int, x), CTRL, 0xf, 0xf, true));
}
template <int CTRL> __device__ __forceinline__ int dppi(int x) { return __builtin_amdgcn_mov_dpp(x, CTRL, 0xf, 0xf, true); }
__device__ __forceinline__ float row_sum(float x) {
  x += dpp<0x128>(x);
  x += dpp<0x124>(x);
  x += dpp<0x122>(x);
  x += dpp<0x121>(x);
  return x;
}
__device__ __forceinline__ float xrow_max(float x) {
  auto s = __builtin_amdgcn_permlane16_swap(__float_as_uint(x), __float_as_uint(x), false, false);
  x = fmaxf(__uint_as_float(s[0]), __uint_as_float(s[1]));
  auto t = __builtin_amdgcn_permlane32_swap(__float_as_uint(x), __float_as_uint(x), false, false);
  return fmaxf(__uint_as_float(t[0]), __uint_as_float(t[1]));
}
__device__ __forceinline__ float xrow_sum(float x) {
  auto s = __builtin_amdgcn_permlane16_swap(__float_as_uint(x), __float_as_uint(x), false, false);
  x = __uint_as_float(s[0]) + __uint_as_float(s[1]);
  auto t = __builtin_amdgcn_permlane32_swap(__float_as_uint(x), __float_as_uint(x), false, false);
  return __uint_as_float(t[0]) + __uint_as_float(t[1]);
}
__device__ __forceinline__ int rfl(int x) { return __builtin_amdgcn_readfirstlane(x); }
#define SB() __builtin_amdgcn_sched_barrier(0)

__global__ void __launch_bounds__(NW * 64, 1)
paged_attn_kernel(const unsigned short* __restrict__ q, const unsigned short* __restrict__ kc,
                  const unsigned short* __restrict__ vc, const int* __restrict__ bt,
                  const int* __restrict__ lens, unsigned short* __restrict__ out, unsigned char* ws, unsigned long long kv_bytes64) {
  __shared__ __attribute__((aligned(16))) int lds[L_END];
  unsigned cpf = 0;
  if (CPF) {
    u64 pc; unsigned off = (threadIdx.x & 63u) * 128u;
    asm volatile("s_getpc_b64 %0\n.Lcpf_here:\n\tv_min_u32 %1, .Lcpf_end-.Lcpf_here, %1" : "=s"(pc), "+v"(off));
    cpf = *reinterpret_cast<const __attribute__((address_space(1))) unsigned*>(pc + off);
    asm volatile("" ::: "memory");
  }
  const int bh = blockIdx.x, b = bh >> 5, h = bh & 31;
  const int tid = threadIdx.x, lane = tid & 63, r = lane >> 4, c = lane & 15;
  const int wave = rfl(tid >> 6);
  constexpr float C2 = 0.088388347648318440f * 1.4426950408889634f;

  unsigned kv_bytes = (unsigned)kv_bytes64;
  asm volatile("" : "+s"(kv_bytes) : "s"(out), "s"(ws));
  int len = lens[b];
  int phys0 = bt[b * NBPS + wave], phys1 = bt[b * NBPS + NS + wave];
  SB();
  unsigned wsl = 0;
  unsigned wso = 0;
  if (WARM == 1) wsl = __hip_atomic_load(reinterpret_cast<const unsigned*>(ws + (size_t)bh * SLOT_BYTES + min(lane, COMPACT ? 9 : 15) * 64), RLX_AGENT);
  if (WARM == 2) wsl = *reinterpret_cast<const unsigned*>(ws + (size_t)bh * SLOT_BYTES + min(lane, COMPACT ? 9 : 15) * 64);
  if (OUTW) wso = __hip_atomic_load(reinterpret_cast<const unsigned*>(out + (size_t)bh * D) + lane, RLX_AGENT);
  int bt0 = bt[b * NBPS + lane], bt1 = bt[b * NBPS + 64 + lane];
  u32x4 qown = QNT ? __builtin_nontemporal_load(reinterpret_cast<const u32x4*>(q + (size_t)bh * D + 8 * c)) : *reinterpret_cast<const u32x4*>(q + (size_t)bh * D + 8 * c);
  int lv = lens[lane & (B - 1)];
  if (tid == 0) *reinterpret_cast<i32x4*>(lds + L_PROG) = i32x4{0, 0, 0, 0};
  const unsigned lbase = (unsigned)r * TOK_BYTES + (unsigned)c * 16u;
  unsigned vb[4]; int rt[4];
#pragma unroll
  for (int t = 0; t < 4; ++t) { vb[t] = lbase + t * 4 * TOK_BYTES; rt[t] = r + 4 * t; asm volatile("" : "+v"(vb[t]), "+v"(rt[t])); }
  SB();
  const bool allm = len <= 0;
  len = allm ? SKV : min(len, SKV);
  u32x4 Ab[4], Bb[4];
#define SLOT(phys_, h_, rem_) ((unsigned)(phys_) * PAGE_BYTES + (unsigned)(h_) * (D * 2) + (unsigned)max(0, min((rem_), PAGE)))
#define RSRC(base_, t_) __builtin_amdgcn_make_buffer_rsrc((void*)(base_), 0, min(((t_) & ~(PAGE_BYTES - 1u)) + ((t_) & 31u) * TOK_BYTES, kv_bytes), 0x00020000)
#define ISSUE(X, rs, so_) do { _Pragma("unroll") for (int t_ = 0; t_ < 4; ++t_) X[t_] = __builtin_amdgcn_raw_buffer_load_b128(rs, vb[t_], so_, AUX); } while (0)
  unsigned tA = SLOT(phys0, h, wave < NS ? len - wave * PAGE : 0), tB = SLOT(phys1, h, wave < NS ? len - (NS + wave) * PAGE : 0);
  ISSUE(Ab, RSRC(kc, tA), tA & ~255u); SB();
  ISSUE(Bb, RSRC(kc, tB), tB & ~255u); SB();
  asm volatile("s_waitcnt lgkmcnt(0)\n\ts_barrier" ::: "memory");

  const int np = (len + PAGE - 1) / PAGE;
  const int d0 = 8 * c + 2 * r;
  int pnext = 0, pend = 0;
  int give = 0;
  int F = 0, dbh = 0, f1n = 0, g1n = 0, n0 = 0;
  u64 rtag = 0, ftag = 0;
  if (wave == NS) {
    const int sj = lane & (B - 1);
    rtag = __hip_atomic_load(reinterpret_cast<u64*>(ws + (size_t)bh * SLOT_BYTES + SLOT_EPOCH), RLX_AGENT);
    const u64 allmask = __ballot(lv <= 0);
    lv = lv <= 0 ? SKV : min(lv, SKV);
    const int npv = (lv + PAGE - 1) / PAGE;
    const int key = lv * B + (B - 1 - sj);
    int rank = 0;
#pragma unroll
    for (int j = 0; j < B; ++j) rank += __builtin_amdgcn_readlane(key, j) > key ? 1 : 0;
    int tot = npv + dppi<0x124>(npv);
    tot += dppi<0x122>(tot);
    tot += dppi<0x121>(tot);
    const int pos = rank < B / 2 ? B - 2 - 2 * rank : 2 * rank - B + 1;
    const int mypos = __builtin_amdgcn_readlane(pos, b);
    const int pb = __builtin_ctzll(__ballot(pos == ((mypos + B - 1) & (B - 1))));
    dbh = pb * H + (h ^ HX);
    const int f0 = bt[pb * NBPS + lane], f1 = bt[pb * NBPS + 64 + lane];
    int qf = QNT ? __builtin_nontemporal_load(reinterpret_cast<const int*>(q) + dbh * (D / 2) + lane) : reinterpret_cast<const int*>(q)[dbh * (D / 2) + lane];
    ftag = __hip_atomic_load(reinterpret_cast<u64*>(ws + (size_t)dbh * SLOT_BYTES + SLOT_EPOCH), RLX_AGENT);
    const int ex = npv - (tot / B + (pos < tot % B ? 1 : 0));
    const int pk = (ex << 3) | pos;
    int pre = 0;
#pragma unroll
    for (int j = 0; j < B; ++j) { const int pj = __builtin_amdgcn_readlane(pk, j); pre += (pj & 7) <= pos ? pj >> 3 : 0; }
    int pmin = min(pre, dppi<0x124>(pre));
    pmin = min(pmin, dppi<0x122>(pmin));
    pmin = min(pmin, dppi<0x121>(pmin));
    const int gv = HELP ? max(0, min(min(pre - pmin, tot * FMAXN / (32 * B * NS)), npv - K0 * NS)) : 0;
    give = __builtin_amdgcn_readlane(gv, b);
    F = __builtin_amdgcn_readlane(gv, pb);
    const int lo = min(np, K0 * NS), own = np - give - lo;
    const int top = max(0, min(own + F + lo - (rfl(tot) + RND) / (B * NS * 2) * (NS * 2), (own + F + lo) / NW));
    const int S = min(top, own), rest = own - S;
    const int pl = __builtin_amdgcn_readlane(lv, pb), pnp = (pl + PAGE - 1) / PAGE;
    f1n = F * (32 * B * NS) > rfl(tot) * FSPLIT ? F / 2 : 0;
    g1n = give * (32 * B * NS) > rfl(tot) * FSPLIT ? give / 2 : 0;
    const int c2 = max(0, min((rest + F + 1) / (2 * NS) * 2 - (F - f1n), rest));
    const int c1 = max(0, min((rest + F + 3) / (2 * NS) * 2 - f1n, rest - c2)), c0 = rest - c1 - c2;
    if (lane == 0) {
      *reinterpret_cast<i32x4*>(lds + L_RNG) = i32x4{lo, lo + c0, 0, 0};
      *reinterpret_cast<i32x4*>(lds + L_RNG + 4) = i32x4{lo + c0, lo + c0 + c1, pnp - F, pnp - F + f1n};
      *reinterpret_cast<i32x4*>(lds + L_RNG + 8) = i32x4{lo + c0 + c1, lo + rest, pnp - F + f1n, pnp};
      lds[L_FP] = pl;
      asm volatile("s_waitcnt lgkmcnt(0)" ::: "memory");
      __hip_atomic_store(lds + L_FLAG, 1, RLX_WG);
    }
    pnext = rfl(lo + rest); pend = rfl(pnext + S); n0 = K0 + c0;
    if ((allmask >> pb) & 1) qf = 0;
    lds[L_FTAB + lane] = f0; lds[L_FTAB + 64 + lane] = f1; lds[L_QF + lane] = qf;
    asm volatile("s_waitcnt lgkmcnt(0)" ::: "memory");
    if (lane == 0) __hip_atomic_store(lds + L_FLAG, 2, RLX_WG);
    __builtin_amdgcn_s_waitcnt(0x0F70);
  }

  float M = -1e30f, L = 0.f, a0 = 0.f, a1 = 0.f;
  float* ldsf = reinterpret_cast<float*>(lds);
  if (DRY || wave < NS || pnext < pend) {
    u32x4 qv = allm ? u32x4{0u, 0u, 0u, 0u} : qown;
    float m = -1e30f, mo = -1e30f, l = 0.f;
    f32x2 acc2[4];
#pragma unroll
    for (int k = 0; k < 4; ++k) acc2[k] = f32x2{0.f, 0.f};
    if (SVLATE && wave >= NS && pnext < pend) while (rfl(__hip_atomic_load(lds + L_PROG, RLX_WG)) < n0 - (pend - pnext) - 2) __builtin_amdgcn_s_sleep(8);
    int fst = 0, cf = 0, no = 64, n = 64, sfill = -1;
    unsigned tK = SLOT(__builtin_amdgcn_ds_bpermute((lane * NS + wave) << 2, bt0), h, lane < K0 && wave < NS ? len - (lane * NS + wave) * PAGE : 0), tV = 0;
    f32x4* sc = reinterpret_cast<f32x4*>(ldsf + L_SC + wave * 64 * PAGE) + r;
#define KSTEP(X, tX, s_) do { \
      const unsigned tn = (s_) + 2 < n ? __builtin_amdgcn_readlane(tK, (s_) + 2) : __builtin_amdgcn_readlane(tV, (s_) + 2 - n); \
      const __amdgpu_buffer_rsrc_t nrs = RSRC((s_) + 2 < n ? kc : vc, tn); \
      if ((s_) == no) { mo = m; m = -1e30f; qv = *reinterpret_cast<const u32x4*>(lds + L_QF + 4 * c); } \
      SB(); \
      float x[4]; \
      _Pragma("unroll") for (int t = 0; t < 4; ++t) { \
        float d = 0.f; \
        _Pragma("unroll") for (int k = 0; k < 4; ++k) d = dot2(X[t][k], qv[k], d); \
        x[t] = d; \
        if (TIGHTK > 0 || (TIGHTK == 0 && t == 3)) { \
          if (TIGHTK) asm volatile("" : "+v"(x[t]) :: "memory"); \
          else asm volatile("" : "+v"(x[0]), "+v"(x[1]), "+v"(x[2]), "+v"(x[3]) :: "memory"); \
          SB(); \
          if (TIGHTK) X[t] = __builtin_amdgcn_raw_buffer_load_b128(nrs, vb[t], tn & ~255u, AUX); else ISSUE(X, nrs, tn & ~255u); \
          asm volatile("" ::: "memory"); \
          SB(); \
        } \
      } \
      _Pragma("unroll") for (int t = 0; t < 4; ++t) { \
        x[t] = row_sum(x[t]) * C2; \
        x[t] = (rt[t] < (int)(tX & 31u)) ? x[t] : -INFINITY; \
        m = fmaxf(m, x[t]); \
      } \
      if (TIGHTK < 0) { \
        asm volatile("" : "+v"(x[0]), "+v"(x[1]), "+v"(x[2]), "+v"(x[3]) :: "memory"); \
        SB(); ISSUE(X, nrs, tn & ~255u); asm volatile("" ::: "memory"); SB(); \
      } \
      if (c == 0) sc[4 * (s_)] = f32x4{x[0], x[1], x[2], x[3]}; \
      tX = tn; \
    } while (0)
#define VSTEP(X, j_) do {        \
      const unsigned tn = __builtin_amdgcn_readlane(tV, (j_) + 2); \
      const __amdgpu_buffer_rsrc_t nrs = RSRC(vc, tn); \
      const f32x4 x = sc[4 * ((j_) < cf ? no + (j_) : (j_) - cf)]; \
      float p[4]; \
      _Pragma("unroll") for (int t = 0; t < 4; ++t) p[t] = __builtin_amdgcn_exp2f(x[t] - m); \
      l += (p[0] + p[1]) + (p[2] + p[3]); \
      SB(); \
      _Pragma("unroll") for (int t = 0; t < 4; ++t) { \
        _Pragma("unroll") for (int k = 0; k < 4; ++k) { \
          const unsigned w = X[t][k]; \
          if (PK) { \
            const f32x2 v2 = {__builtin_bit_cast(float, w << 16), __builtin_bit_cast(float, w & 0xffff0000u)}; \
            acc2[k] = __builtin_elementwise_fma(f32x2{p[t], p[t]}, v2, acc2[k]); \
          } else { \
            acc2[k][0] = fmaf(p[t], __builtin_bit_cast(float, w << 16), acc2[k][0]); \
            acc2[k][1] = fmaf(p[t], __builtin_bit_cast(float, w & 0xffff0000u), acc2[k][1]); \
          } \
        } \
        if (TIGHTV || t == 3) { \
          asm volatile("" : "+v"(acc2[0]), "+v"(acc2[1]), "+v"(acc2[2]), "+v"(acc2[3]) :: "memory"); \
          SB(); \
          if (TIGHTV) X[t] = __builtin_amdgcn_raw_buffer_load_b128(nrs, vb[t], tn & ~255u, AUX); else ISSUE(X, nrs, tn & ~255u); \
          asm volatile("" ::: "memory"); \
          SB(); \
        } \
      } \
    } while (0)
#define FLUSH() do { \
      M = xrow_max(m); \
      const float sc_ = __builtin_amdgcn_exp2f(m - M); \
      L = xrow_sum(l * sc_); \
      _Pragma("unroll") for (int k = 0; k < 4; ++k) { acc2[k][0] = xrow_sum(acc2[k][0] * sc_); acc2[k][1] = xrow_sum(acc2[k][1] * sc_); } \
      a0 = r == 0 ? acc2[0][0] : r == 1 ? acc2[1][0] : r == 2 ? acc2[2][0] : acc2[3][0]; \
      a1 = r == 0 ? acc2[0][1] : r == 1 ? acc2[1][1] : r == 2 ? acc2[2][1] : acc2[3][1]; } while (0)
#define PUBLISH() do { \
      float* srec = ldsf + (NW + wave) * REC; \
      *reinterpret_cast<f32x2*>(srec + 4 + d0) = f32x2{a0, a1}; \
      if (lane == 0) *reinterpret_cast<f32x2*>(srec) = f32x2{M, L}; \
      asm volatile("s_waitcnt lgkmcnt(0)" ::: "memory"); \
      if (lane == 0) __hip_atomic_store(lds + L_SEND + wave - 1, 1, RLX_WG); \
      m = mo; l = 0.f; \
      _Pragma("unroll") for (int k = 0; k < 4; ++k) acc2[k] = f32x2{0.f, 0.f}; } while (0)
    for (int s = 0; s < n; s += 2) {
      s = rfl(s);
      if (tid == 0) __hip_atomic_store(lds + L_PROG, s, RLX_WG);
      if (s == K0 - 2) {
        if (wave < NS) {
          while (rfl(__hip_atomic_load(lds + L_FLAG, RLX_WG)) < 1) __builtin_amdgcn_s_sleep(1);
          asm volatile("" ::: "memory");
          const i32x4 rg = *reinterpret_cast<const i32x4*>(lds + L_RNG + 4 * wave);
          pnext = rfl(rg[0]); pend = rfl(rg[1]); fst = rfl(rg[2]); cf = rfl(rg[3]) - fst;
        }
        no = K0 + pend - pnext + ((pend - pnext + cf) & 1); n = no + cf;
        sfill = no >= K0 + 2 ? K0 : K0 - 2;
        const int pn = pnext + lane - K0;
        const int p0 = __builtin_amdgcn_ds_bpermute(pn << 2, bt0), p1 = __builtin_amdgcn_ds_bpermute(pn << 2, bt1);
        if (lane >= K0) tK = pn < pend ? SLOT((pn & 64) ? p1 : p0, h, len - pn * PAGE) : 0u;
      }
      if (s == sfill) {
        if (cf > 0) {
          while (rfl(__hip_atomic_load(lds + L_FLAG, RLX_WG)) < 2) __builtin_amdgcn_s_sleep(1);
          asm volatile("" ::: "memory");
          const int pn = fst + lane - no;
          if (lane >= no && lane < n) tK = SLOT(lds[L_FTAB + pn], h ^ HX, lds[L_FP] - pn * PAGE);
        }
        tV = __builtin_amdgcn_ds_bpermute((lane < cf ? no + lane : lane < n ? lane - cf : 63) << 2, tK);
      }
      KSTEP(Ab, tA, s);
      KSTEP(Bb, tB, s + 1);
    }
    for (int j = 0;; j += 2) {
      j = rfl(j);
      if (tid == 0) __hip_atomic_store(lds + L_PROG, n + j, RLX_WG);
      VSTEP(Ab, j);
      if (__builtin_expect(j + 1 == cf, 0)) { FLUSH(); PUBLISH(); }
      VSTEP(Bb, j + 1);
      const bool fin = j + 2 == n;
      if (j + 2 == cf || fin) {
        FLUSH();
        if (fin) break;
        PUBLISH();
      }
    }
    if (tid == 0) __hip_atomic_store(lds + L_PROG, 1 << 20, RLX_WG);
  }
  if (wave == NS) {
    const __amdgpu_buffer_rsrc_t wrs = __builtin_amdgcn_make_buffer_rsrc(ws, 0, B * H * SLOT_BYTES, 0x00020000);
    const unsigned loff = lane < REC / 4 ? lane * 16u : OOB;
    u32x4* lrec = reinterpret_cast<u32x4*>(lds + NW * REC) + min(lane, REC / 4 - 1);
    if (give <= 0) *lrec = u32x4{lane ? 0u : __float_as_uint(-1e30f), 0u, 0u, 0u};
    bool send = F > 0, recv = give > 0;
    for (unsigned spins = 0; send || recv; ++spins) {
      const int prog = rfl(__hip_atomic_load(lds + L_PROG, RLX_WG));
      const int s1 = rfl(__hip_atomic_load(lds + L_SEND, RLX_WG)), s2 = rfl(__hip_atomic_load(lds + L_SEND + 1, RLX_WG));
      if (send && s2 && (s1 || f1n == 0)) {
        asm volatile("" ::: "memory");
        f32x4 rec2 = *reinterpret_cast<const f32x4*>(lrec + 2 * (REC / 4));
        if (f1n) {
          const f32x4 rec1 = *reinterpret_cast<const f32x4*>(lrec + REC / 4);
          const float m1 = ldsf[(NW + 1) * REC], m2 = ldsf[(NW + 2) * REC], mm = fmaxf(m1, m2);
          const float e1 = __builtin_amdgcn_exp2f(m1 - mm), e2 = __builtin_amdgcn_exp2f(m2 - mm);
          rec2 = rec1 * e1 + rec2 * e2;
          if (lane == 0) rec2[0] = mm;
        }
        __builtin_amdgcn_raw_buffer_store_b128(__builtin_bit_cast(u32x4, rec2), wrs, loff, (unsigned)rfl(dbh) * SLOT_BYTES, 16);
        asm volatile("s_waitcnt vmcnt(0)" ::: "memory");
        if (lane == 0) __hip_atomic_store(reinterpret_cast<u64*>(ws + (size_t)dbh * SLOT_BYTES + SLOT_FLAG), ftag + 1u, RLX_AGENT);
        send = false;
      }
      if (recv && prog >= n0 + give - g1n + PWAIT) {
        const u64 fl = __hip_atomic_load(reinterpret_cast<u64*>(ws + (size_t)bh * SLOT_BYTES + SLOT_FLAG), RLX_AGENT);
        if (__all(fl == rtag + 1u)) {
          asm volatile("" ::: "memory");
          const u32x4 pay = __builtin_amdgcn_raw_buffer_load_b128(wrs, loff, (unsigned)bh * SLOT_BYTES, 16);
          if (lane < REC / 4) *lrec = pay;
          asm volatile("s_waitcnt vmcnt(0)" ::: "memory");
          if (lane == 0) __hip_atomic_store(reinterpret_cast<u64*>(ws + (size_t)bh * SLOT_BYTES + SLOT_EPOCH), rtag + 1u, RLX_AGENT);
          recv = false;
        } else if (spins > (1u << 23)) {
          *lrec = u32x4{0u, 0x7fc00000u, 0u, 0u};
          recv = false;
        }
      }
      __builtin_amdgcn_s_sleep(SLP);
    }
  }
  float* rec = ldsf + wave * REC;
  *reinterpret_cast<f32x2*>(rec + 4 + d0) = f32x2{a0, a1};
  if (lane == 0) *reinterpret_cast<f32x2*>(rec) = f32x2{M, L};
  if (CPF) asm volatile("" :: "v"(cpf), "v"(wsl), "v"(wso));
  __syncthreads();
  if (tid < 64) {
    float mw[NW + 1], lw[NW + 1], MM = -1e30f;
#pragma unroll
    for (int w = 0; w <= NW; ++w) {
      const f32x2 ml = *reinterpret_cast<const f32x2*>(ldsf + w * REC);
      mw[w] = ml[0]; lw[w] = ml[1];
      MM = fmaxf(MM, mw[w]);
    }
    float LL = 0.f, o0 = 0.f, o1 = 0.f;
#pragma unroll
    for (int w = 0; w <= NW; ++w) {
      const float s = __builtin_amdgcn_exp2f(mw[w] - MM);
      const f32x2 a = *reinterpret_cast<const f32x2*>(ldsf + w * REC + 4 + 2 * tid);
      LL = fmaf(s, lw[w], LL); o0 = fmaf(s, a[0], o0); o1 = fmaf(s, a[1], o1);
    }
    const float inv = RCP ? __builtin_amdgcn_rcpf(LL) : 1.f / LL;
    const __hip_bfloat16 r0 = __float2bfloat16(o0 * inv), r1 = __float2bfloat16(o1 * inv);
    const unsigned pk = (unsigned)__builtin_bit_cast(unsigned short, r0) | ((unsigned)__builtin_bit_cast(unsigned short, r1) << 16);
    if (CPF) asm volatile(".Lcpf_end:");
    *reinterpret_cast<unsigned*>(out + (size_t)bh * D + 2 * tid) = pk;
  }
}
}

extern "C" void kernel_launch(void* const* d_in, const int* in_sizes, int n_in, void* d_out, int out_size,
                              void* d_ws, size_t ws_size, hipStream_t stream) {
  const unsigned long long kv_bytes = (unsigned long long)in_sizes[1] * 2ull;
  paged_attn_kernel<<<B * H, NW * 64, 0, stream>>>(
      (const unsigned short*)d_in[0], (const unsigned short*)d_in[1], (const unsigned short*)d_in[2],
      (const int*)d_in[3], (const int*)d_in[4], (unsigned short*)d_out, (unsigned char*)d_ws, kv_bytes);
}
```

```cpp
#include <hip/hip_runtime.h>
#include <hip/hip_bf16.h>
#include <stdint.h>

namespace {
constexpr int B = 8, H = 32, D = 128, PAGE = 16, NBPS = 128, SKV = NBPS * PAGE;
constexpr int NS = 3, NW = NS + 1;
constexpr int K0 = 4;
constexpr int HX = 2;
constexpr int FMAXN = 48, FSPLIT = 12;
constexpr int PWAIT = 6;
constexpr int AUX_NT = 2, AUX_SC1 = 16;
constexpr unsigned TOK_BYTES = H * D * 2;
constexpr unsigned PAGE_BYTES = PAGE * TOK_BYTES;
constexpr int REC = D + 4;
constexpr unsigned SLOT_BYTES = 1024, SLOT_FLAG = 544, SLOT_EPOCH = 576, SLOT_USED = 640;
constexpr int L_PROG = (NW + 3) * REC, L_FLAG = L_PROG + 1, L_SEND = L_PROG + 2, L_RNG = L_PROG + 4, L_FP = L_RNG + 4 * NS,
              L_FTAB = L_FP + 4, L_QF = L_FTAB + NBPS, L_SC = L_QF + 64, L_END = L_SC + NW * 64 * PAGE;

using bf16x2 = __attribute__((ext_vector_type(2))) __bf16;
using u32x4 = __attribute__((ext_vector_type(4))) unsigned;
using i32x4 = __attribute__((ext_vector_type(4))) int;
using f32x2 = __attribute__((ext_vector_type(2))) float;
using f32x4 = __attribute__((ext_vector_type(4))) float;
typedef unsigned long long u64;
#define RLX_AGENT __ATOMIC_RELAXED, __HIP_MEMORY_SCOPE_AGENT
#define RLX_WG __ATOMIC_RELAXED, __HIP_MEMORY_SCOPE_WORKGROUP
#define SB() __builtin_amdgcn_sched_barrier(0)

__device__ __forceinline__ float dot2(unsigned k, unsigned q, float acc) {
  return __builtin_amdgcn_fdot2_f32_bf16(__builtin_bit_cast(bf16x2, k), __builtin_bit_cast(bf16x2, q), acc, false);
}
template <int CTRL> __device__ __forceinline__ float dpp(float x) {
  return __builtin_bit_cast(float, __builtin_amdgcn_mov_dpp(__builtin_bit_cast(int, x), CTRL, 0xf, 0xf, true));
}
template <int CTRL> __device__ __forceinline__ int dppi(int x) { return __builtin_amdgcn_mov_dpp(x, CTRL, 0xf, 0xf, true); }
__device__ __forceinline__ float row_sum(float x) {
  x += dpp<0x128>(x);
  x += dpp<0x124>(x);
  x += dpp<0x122>(x);
  x += dpp<0x121>(x);
  return x;
}
__device__ __forceinline__ float xrow_max(float x) {
  auto s = __builtin_amdgcn_permlane16_swap(__float_as_uint(x), __float_as_uint(x), false, false);
  x = fmaxf(__uint_as_float(s[0]), __uint_as_float(s[1]));
  auto t = __builtin_amdgcn_permlane32_swap(__float_as_uint(x), __float_as_uint(x), false, false);
  return fmaxf(__uint_as_float(t[0]), __uint_as_float(t[1]));
}
__device__ __forceinline__ float xrow_sum(float x) {
  auto s = __builtin_amdgcn_permlane16_swap(__float_as_uint(x), __float_as_uint(x), false, false);
  x = __uint_as_float(s[0]) + __uint_as_float(s[1]);
  auto t = __builtin_amdgcn_permlane32_swap(__float_as_uint(x), __float_as_uint(x), false, false);
  return __uint_as_float(t[0]) + __uint_as_float(t[1]);
}
__device__ __forceinline__ int rfl(int x) { return __builtin_amdgcn_readfirstlane(x); }

__global__ void __launch_bounds__(NW * 64, 1)
paged_attn_kernel(const unsigned short* __restrict__ q, const unsigned short* __restrict__ kc,
                  const unsigned short* __restrict__ vc, const int* __restrict__ bt,
                  const int* __restrict__ lens, unsigned short* __restrict__ out, unsigned char* ws, unsigned long long kv_bytes64) {
  __shared__ __attribute__((aligned(16))) int lds[L_END];
  unsigned cpf;
  {
    u64 pc; unsigned off = (threadIdx.x & 63u) * 128u;
    asm volatile("s_getpc_b64 %0\n.Lcpf_here:\n\tv_min_u32 %1, .Lcpf_end-.Lcpf_here, %1" : "=s"(pc), "+v"(off));
    cpf = *reinterpret_cast<const __attribute__((address_space(1))) unsigned*>(pc + off);
    asm volatile("" ::: "memory");
  }
  const int bh = blockIdx.x, b = bh >> 5, h = bh & 31;
  const int tid = threadIdx.x, lane = tid & 63, r = lane >> 4, c = lane & 15;
  const int wave = rfl(tid >> 6);
  constexpr float C2 = 0.088388347648318440f * 1.4426950408889634f;

  unsigned kv_bytes = (unsigned)kv_bytes64;
  asm volatile("" : "+s"(kv_bytes) : "s"(vc), "s"(out), "s"(ws));
  int len = lens[b];
  const int phys0 = bt[b * NBPS + wave], phys1 = bt[b * NBPS + NS + wave];
  SB();
  const unsigned wsl = __hip_atomic_load(reinterpret_cast<const unsigned*>(ws + (size_t)bh * SLOT_BYTES + min(lane * 64u, SLOT_USED - 64u)), RLX_AGENT);
  const int bt0 = bt[b * NBPS + lane], bt1 = bt[b * NBPS + 64 + lane];
  const u32x4 qown = __builtin_nontemporal_load(reinterpret_cast<const u32x4*>(q + (size_t)bh * D + 8 * c));
  int lv = lens[lane & (B - 1)];
  if (tid == 0) *reinterpret_cast<i32x4*>(lds + L_PROG) = i32x4{0, 0, 0, 0};
  unsigned vb[4]; int rt[4];
#pragma unroll
  for (int t = 0; t < 4; ++t) {
    rt[t] = r + 4 * t; vb[t] = (unsigned)rt[t] * TOK_BYTES + (unsigned)c * 16u;
    asm volatile("" : "+v"(vb[t]), "+v"(rt[t]));
  }
  SB();
  const bool allm = len <= 0;
  len = allm ? SKV : min(len, SKV);
  u32x4 Ab[4], Bb[4];
#define SLOT(phys_, h_, rem_) ((unsigned)(phys_) * PAGE_BYTES + (unsigned)(h_) * (D * 2) + (unsigned)max(0, min((rem_), PAGE)))
#define RSRC(base_, t_) __builtin_amdgcn_make_buffer_rsrc((void*)(base_), 0, min(((t_) & ~(PAGE_BYTES - 1u)) + ((t_) & 31u) * TOK_BYTES, kv_bytes), 0x00020000)
#define ISSUE(X, rs, so_) do { _Pragma("unroll") for (int t_ = 0; t_ < 4; ++t_) X[t_] = __builtin_amdgcn_raw_buffer_load_b128(rs, vb[t_], so_, AUX_NT); } while (0)
  unsigned tA = SLOT(phys0, h, wave < NS ? len - wave * PAGE : 0), tB = SLOT(phys1, h, wave < NS ? len - (NS + wave) * PAGE : 0);
  ISSUE(Ab, RSRC(kc, tA), tA & ~255u); SB();
  ISSUE(Bb, RSRC(kc, tB), tB & ~255u); SB();
  asm volatile("s_waitcnt lgkmcnt(0)\n\ts_barrier" ::: "memory");

  const int np = (len + PAGE - 1) / PAGE;
  const int d0 = 8 * c + 2 * r;
  int pnext = 0, pend = 0;
  int give = 0, F = 0;
  int dbh = 0, f1n = 0, g1n = 0, n0 = 0;
  u64 rtag = 0, ftag = 0;
  if (wave == NS) {
    const int sj = lane & (B - 1);
    rtag = __hip_atomic_load(reinterpret_cast<u64*>(ws + (size_t)bh * SLOT_BYTES + SLOT_EPOCH), RLX_AGENT);
    const u64 allmask = __ballot(lv <= 0);
    lv = lv <= 0 ? SKV : min(lv, SKV);
    const int npv = (lv + PAGE - 1) / PAGE;
    const int key = lv * B + (B - 1 - sj);
    int rank = 0;
#pragma unroll
    for (int j = 0; j < B; ++j) rank += __builtin_amdgcn_readlane(key, j) > key ? 1 : 0;
    int tot = npv + dppi<0x124>(npv);
    tot += dppi<0x122>(tot);
    tot += dppi<0x121>(tot);
    const int pos = rank < B / 2 ? B - 2 - 2 * rank : 2 * rank - B + 1;
    const int mypos = __builtin_amdgcn_readlane(pos, b);
    const int pb = __builtin_ctzll(__ballot(pos == ((mypos + B - 1) & (B - 1))));
    dbh = pb * H + (h ^ HX);
    const int f0 = bt[pb * NBPS + lane], f1 = bt[pb * NBPS + 64 + lane];
    int qf = __builtin_nontemporal_load(reinterpret_cast<const int*>(q) + dbh * (D / 2) + lane);
    ftag = __hip_atomic_load(reinterpret_cast<u64*>(ws + (size_t)dbh * SLOT_BYTES + SLOT_EPOCH), RLX_AGENT);
    const int ex = npv - (tot / B + (pos < tot % B ? 1 : 0));
    const int pk = (ex << 3) | pos;
    int pre = 0;
#pragma unroll
    for (int j = 0; j < B; ++j) { const int pj = __builtin_amdgcn_readlane(pk, j); pre += (pj & 7) <= pos ? pj >> 3 : 0; }
    int pmin = min(pre, dppi<0x124>(pre));
    pmin = min(pmin, dppi<0x122>(pmin));
    pmin = min(pmin, dppi<0x121>(pmin));
    const int gv = max(0, min(min(pre - pmin, tot * FMAXN / (32 * B * NS)), npv - K0 * NS));
    give = __builtin_amdgcn_readlane(gv, b);
    F = __builtin_amdgcn_readlane(gv, pb);
    const int lo = min(np, K0 * NS), own = np - give - lo;
    const int top = max(0, min(own + F + lo - (rfl(tot) + B * NS * 2 - 1) / (B * NS * 2) * (NS * 2), (own + F + lo) / NW));
    const int S = min(top, own), rest = own - S;
    const int pl = __builtin_amdgcn_readlane(lv, pb), pnp = (pl + PAGE - 1) / PAGE;
    f1n = F * (32 * B * NS) > rfl(tot) * FSPLIT ? F / 2 : 0;
    g1n = give * (32 * B * NS) > rfl(tot) * FSPLIT ? give / 2 : 0;
    const int c2 = max(0, min((rest + F + 1) / (2 * NS) * 2 - (F - f1n), rest));
    const int c1 = max(0, min((rest + F + 3) / (2 * NS) * 2 - f1n, rest - c2)), c0 = rest - c1 - c2;
    if (lane == 0) {
      *reinterpret_cast<i32x4*>(lds + L_RNG) = i32x4{lo, lo + c0, 0, 0};
      *reinterpret_cast<i32x4*>(lds + L_RNG + 4) = i32x4{lo + c0, lo + c0 + c1, pnp - F, pnp - F + f1n};
      *reinterpret_cast<i32x4*>(lds + L_RNG + 8) = i32x4{lo + c0 + c1, lo + rest, pnp - F + f1n, pnp};
      lds[L_FP] = pl;
      asm volatile("s_waitcnt lgkmcnt(0)" ::: "memory");
      __hip_atomic_store(lds + L_FLAG, 1, RLX_WG);
    }
    pnext = rfl(lo + rest); pend = rfl(pnext + S); n0 = K0 + c0;
    if ((allmask >> pb) & 1) qf = 0;
    lds[L_FTAB + lane] = f0; lds[L_FTAB + 64 + lane] = f1; lds[L_QF + lane] = qf;
    asm volatile("s_waitcnt lgkmcnt(0)" ::: "memory");
    if (lane == 0) __hip_atomic_store(lds + L_FLAG, 2, RLX_WG);
    __builtin_amdgcn_s_waitcnt(0x0F70);
  }

  float M = -1e30f, L = 0.f, a0 = 0.f, a1 = 0.f;
  float* ldsf = reinterpret_cast<float*>(lds);
  {
    u32x4 qv = allm ? u32x4{0u, 0u, 0u, 0u} : qown;
    float m = -1e30f, mo = -1e30f, l = 0.f;
    f32x2 acc2[4];
#pragma unroll
    for (int k = 0; k < 4; ++k) acc2[k] = f32x2{0.f, 0.f};
    if (wave >= NS && pnext < pend)
      while (rfl(__hip_atomic_load(lds + L_PROG, RLX_WG)) < n0 - (pend - pnext) - 2) __builtin_amdgcn_s_sleep(8);
    int fst = 0, cf = 0, no = 64, n = 64, sfill = -1;
    unsigned tK = SLOT(__builtin_amdgcn_ds_bpermute((lane * NS + wave) << 2, bt0), h, lane < K0 && wave < NS ? len - (lane * NS + wave) * PAGE : 0), tV = 0;
    f32x4* sc = reinterpret_cast<f32x4*>(ldsf + L_SC + wave * 64 * PAGE) + r;
#define KSTEP(X, tX, s_) do { \
      const unsigned tn = (s_) + 2 < n ? __builtin_amdgcn_readlane(tK, (s_) + 2) : __builtin_amdgcn_readlane(tV, (s_) + 2 - n); \
      const __amdgpu_buffer_rsrc_t nrs = RSRC((s_) + 2 < n ? kc : vc, tn); \
      if ((s_) == no) { mo = m; m = -1e30f; qv = *reinterpret_cast<const u32x4*>(lds + L_QF + 4 * c); } \
      SB(); \
      float x[4]; \
      _Pragma("unroll") for (int t = 0; t < 4; ++t) { \
        float d = 0.f; \
        _Pragma("unroll") for (int k = 0; k < 4; ++k) d = dot2(X[t][k], qv[k], d); \
        x[t] = d; \
      } \
      asm volatile("" : "+v"(x[0]), "+v"(x[1]), "+v"(x[2]), "+v"(x[3]) :: "memory"); \
      SB(); \
      ISSUE(X, nrs, tn & ~255u); \
      asm volatile("" ::: "memory"); \
      SB(); \
      _Pragma("unroll") for (int t = 0; t < 4; ++t) { \
        x[t] = row_sum(x[t]) * C2; \
        x[t] = (rt[t] < (int)(tX & 31u)) ? x[t] : -INFINITY; \
        m = fmaxf(m, x[t]); \
      } \
      if (c == 0) sc[4 * (s_)] = f32x4{x[0], x[1], x[2], x[3]}; \
      tX = tn; \
    } while (0)
#define VSTEP(X, j_) do { \
      const unsigned tn = __builtin_amdgcn_readlane(tV, (j_) + 2); \
      const __amdgpu_buffer_rsrc_t nrs = RSRC(vc, tn); \
      const f32x4 x = sc[4 * ((j_) < cf ? no + (j_) : (j_) - cf)]; \
      float p[4]; \
      _Pragma("unroll") for (int t = 0; t < 4; ++t) p[t] = __builtin_amdgcn_exp2f(x[t] - m); \
      l += (p[0] + p[1]) + (p[2] + p[3]); \
      SB(); \
      _Pragma("unroll") for (int t = 0; t < 4; ++t) { \
        _Pragma("unroll") for (int k = 0; k < 4; ++k) { \
          const unsigned w = X[t][k]; \
          const f32x2 v2 = {__builtin_bit_cast(float, w << 16), __builtin_bit_cast(float, w & 0xffff0000u)}; \
          acc2[k] = __builtin_elementwise_fma(f32x2{p[t], p[t]}, v2, acc2[k]); \
        } \
        asm volatile("" : "+v"(acc2[0]), "+v"(acc2[1]), "+v"(acc2[2]), "+v"(acc2[3]) :: "memory"); \
        SB(); \
        X[t] = __builtin_amdgcn_raw_buffer_load_b128(nrs, vb[t], tn & ~255u, AUX_NT); \
        asm volatile("" ::: "memory"); \
        SB(); \
      } \
    } while (0)
#define FLUSH() do { \
      M = m; \
      L = xrow_sum(l); \
      float s4[4]; \
      _Pragma("unroll") for (int k = 0; k < 4; ++k) {       \
        auto w_ = __builtin_amdgcn_permlane32_swap(__float_as_uint(acc2[k >> 1][k & 1]), __float_as_uint(acc2[2 + (k >> 1)][k & 1]), false, false); \
        s4[k] = __uint_as_float(w_[0]) + __uint_as_float(w_[1]); } \
      auto u0_ = __builtin_amdgcn_permlane16_swap(__float_as_uint(s4[0]), __float_as_uint(s4[2]), false, false); \
      auto u1_ = __builtin_amdgcn_permlane16_swap(__float_as_uint(s4[1]), __float_as_uint(s4[3]), false, false); \
      a0 = __uint_as_float(u0_[0]) + __uint_as_float(u0_[1]); \
      a1 = __uint_as_float(u1_[0]) + __uint_as_float(u1_[1]); } while (0)
#define PUBLISH() do { \
      float* srec = ldsf + (NW + wave) * REC; \
      *reinterpret_cast<f32x2*>(srec + 4 + d0) = f32x2{a0, a1}; \
      if (lane == 0) *reinterpret_cast<f32x2*>(srec) = f32x2{M, L}; \
      asm volatile("s_waitcnt lgkmcnt(0)" ::: "memory"); \
      if (lane == 0) __hip_atomic_store(lds + L_SEND + wave - 1, 1, RLX_WG); \
      m = mo; l = 0.f; \
      _Pragma("unroll") for (int k = 0; k < 4; ++k) acc2[k] = f32x2{0.f, 0.f}; } while (0)
    for (int s = 0; s < n; s += 2) {
      s = rfl(s);
      if (tid == 0) __hip_atomic_store(lds + L_PROG, s, RLX_WG);
      if (s == K0 - 2) {
        if (wave < NS) {
          while (rfl(__hip_atomic_load(lds + L_FLAG, RLX_WG)) < 1) __builtin_amdgcn_s_sleep(1);
          asm volatile("" ::: "memory");
          const i32x4 rg = *reinterpret_cast<const i32x4*>(lds + L_RNG + 4 * wave);
          pnext = rfl(rg[0]); pend = rfl(rg[1]); fst = rfl(rg[2]); cf = rfl(rg[3]) - fst;
        }
        no = K0 + pend - pnext + ((pend - pnext + cf) & 1); n = no + cf;
        sfill = no >= K0 + 2 ? K0 : K0 - 2;
        const int pn = pnext + lane - K0;
        const int p0 = __builtin_amdgcn_ds_bpermute(pn << 2, bt0), p1 = __builtin_amdgcn_ds_bpermute(pn << 2, bt1);
        if (lane >= K0) tK = pn < pend ? SLOT((pn & 64) ? p1 : p0, h, len - pn * PAGE) : 0u;
      }
      if (s == sfill) {
        if (cf > 0) {
          while (rfl(__hip_atomic_load(lds + L_FLAG, RLX_WG)) < 2) __builtin_amdgcn_s_sleep(1);
          asm volatile("" ::: "memory");
          const int pn = fst + lane - no;
          if (lane >= no && lane < n) tK = SLOT(lds[L_FTAB + pn], h ^ HX, lds[L_FP] - pn * PAGE);
        }
        tV = __builtin_amdgcn_ds_bpermute((lane < cf ? no + lane : lane < n ? lane - cf : 63) << 2, tK);
      }
      KSTEP(Ab, tA, s);
      KSTEP(Bb, tB, s + 1);
    }
    m = xrow_max(m); mo = xrow_max(mo);
    for (int j = 0;; j += 2) {
      j = rfl(j);
      if (tid == 0) __hip_atomic_store(lds + L_PROG, n + j, RLX_WG);
      VSTEP(Ab, j);
      if (__builtin_expect(j + 1 == cf, 0)) { FLUSH(); PUBLISH(); }
      VSTEP(Bb, j + 1);
      const bool fin = j + 2 == n;
      if (j + 2 == cf || fin) {
        FLUSH();
        if (fin) break;
        PUBLISH();
      }
    }
    if (tid == 0) __hip_atomic_store(lds + L_PROG, 1 << 20, RLX_WG);
  }
  if (wave == NS) {
    const __amdgpu_buffer_rsrc_t wrs = __builtin_amdgcn_make_buffer_rsrc(ws, 0, B * H * SLOT_BYTES, 0x00020000);
    const unsigned loff = lane < REC / 4 ? lane * 16u : 0x80000000u;
    u32x4* lrec = reinterpret_cast<u32x4*>(lds + NW * REC) + min(lane, REC / 4 - 1);
    if (give <= 0) *lrec = u32x4{lane ? 0u : __float_as_uint(-1e30f), 0u, 0u, 0u};
    bool send = F > 0, recv = give > 0;
    for (unsigned spins = 0; send || recv; ++spins) {
      const int prog = rfl(__hip_atomic_load(lds + L_PROG, RLX_WG));
      const int s1 = rfl(__hip_atomic_load(lds + L_SEND, RLX_WG)), s2 = rfl(__hip_atomic_load(lds + L_SEND + 1, RLX_WG));
      if (send && s2 && (s1 || f1n == 0)) {
        asm volatile("" ::: "memory");
        f32x4 rec2 = *reinterpret_cast<const f32x4*>(lrec + 2 * (REC / 4));
        if (f1n) {
          const f32x4 rec1 = *reinterpret_cast<const f32x4*>(lrec + REC / 4);
          const float m1 = ldsf[(NW + 1) * REC], m2 = ldsf[(NW + 2) * REC], mm = fmaxf(m1, m2);
          const float e1 = __builtin_amdgcn_exp2f(m1 - mm), e2 = __builtin_amdgcn_exp2f(m2 - mm);
          rec2 = rec1 * e1 + rec2 * e2;
          if (lane == 0) rec2[0] = mm;
        }
        __builtin_amdgcn_raw_buffer_store_b128(__builtin_bit_cast(u32x4, rec2), wrs, loff, (unsigned)rfl(dbh) * SLOT_BYTES, AUX_SC1);
        asm volatile("s_waitcnt vmcnt(0)" ::: "memory");
        if (lane == 0) __hip_atomic_store(reinterpret_cast<u64*>(ws + (size_t)dbh * SLOT_BYTES + SLOT_FLAG), ftag + 1u, RLX_AGENT);
        send = false;
      }
      if (recv && prog >= n0 + give - g1n + PWAIT) {
        const u64 fl = __hip_atomic_load(reinterpret_cast<u64*>(ws + (size_t)bh * SLOT_BYTES + SLOT_FLAG), RLX_AGENT);
        if (__all(fl == rtag + 1u)) {
          __builtin_amdgcn_fence(__ATOMIC_ACQUIRE, "agent");
          asm volatile("" ::: "memory");
          const u32x4 pay = __builtin_amdgcn_raw_buffer_load_b128(wrs, loff, (unsigned)bh * SLOT_BYTES, AUX_SC1);
          if (lane < REC / 4) *lrec = pay;
          asm volatile("s_waitcnt vmcnt(0)" ::: "memory");
          if (lane == 0) __hip_atomic_store(reinterpret_cast<u64*>(ws + (size_t)bh * SLOT_BYTES + SLOT_EPOCH), rtag + 1u, RLX_AGENT);
          recv = false;
        } else if (spins > (1u << 23)) {
          *lrec = u32x4{0u, 0x7fc00000u, 0u, 0u};
          recv = false;
        }
      }
      __builtin_amdgcn_s_sleep(6);
    }
  }
  float* rec = ldsf + wave * REC;
  *reinterpret_cast<f32x2*>(rec + 4 + d0) = f32x2{a0, a1};
  if (lane == 0) *reinterpret_cast<f32x2*>(rec) = f32x2{M, L};
  asm volatile("" :: "v"(cpf), "v"(wsl));
  __syncthreads();
  if (tid < 64) {
    float mw[NW + 1], lw[NW + 1], MM = -1e30f;
#pragma unroll
    for (int w = 0; w <= NW; ++w) {
      const f32x2 ml = *reinterpret_cast<const f32x2*>(ldsf + w * REC);
      mw[w] = ml[0]; lw[w] = ml[1];
      MM = fmaxf(MM, mw[w]);
    }
    float LL = 0.f, o0 = 0.f, o1 = 0.f;
#pragma unroll
    for (int w = 0; w <= NW; ++w) {
      const float s = __builtin_amdgcn_exp2f(mw[w] - MM);
      const f32x2 a = *reinterpret_cast<const f32x2*>(ldsf + w * REC + 4 + 2 * tid);
      LL = fmaf(s, lw[w], LL); o0 = fmaf(s, a[0], o0); o1 = fmaf(s, a[1], o1);
    }
    const float inv = __builtin_amdgcn_rcpf(LL);
    const __hip_bfloat16 r0 = __float2bfloat16(o0 * inv), r1 = __float2bfloat16(o1 * inv);
    const unsigned pk = (unsigned)__builtin_bit_cast(unsigned short, r0) | ((unsigned)__builtin_bit_cast(unsigned short, r1) << 16);
    asm volatile(".Lcpf_end:");
    *reinterpret_cast<unsigned*>(out + (size_t)bh * D + 2 * tid) = pk;
  }
}
}

extern "C" void kernel_launch(void* const* d_in, const int* in_sizes, int n_in, void* d_out, int out_size,
                              void* d_ws, size_t ws_size, hipStream_t stream) {
  const unsigned long long kv_bytes = (unsigned long long)in_sizes[1] * 2ull;
  paged_attn_kernel<<<B * H, NW * 64, 0, stream>>>(
      (const unsigned short*)d_in[0], (const unsigned short*)d_in[1], (const unsigned short*)d_in[2],
      (const int*)d_in[3], (const int*)d_in[4], (unsigned short*)d_out, (unsigned char*)d_ws, kv_bytes);
}
```

```cpp
#include <hip/hip_runtime.h>
#include <hip/hip_bf16.h>
#include <stdint.h>

namespace {
constexpr int B = 8, H = 32, D = 128, PAGE = 16, NBPS = 128, SKV = NBPS * PAGE;
constexpr int NS = 3, NW = NS + 1;
constexpr int K0 = 4;
constexpr int HX = 2;
constexpr int FMAXN = 48, FSPLIT = 12;
constexpr int PWAIT = 6;
#define BWN 7700
#define T0NS 500
constexpr double PACE_GBS = BWN;
constexpr double PACE_T0_NS = T0NS;
constexpr int AUX_NT = 2, AUX_SC1 = 16;
constexpr unsigned TOK_BYTES = H * D * 2;
constexpr unsigned PAGE_BYTES = PAGE * TOK_BYTES;
constexpr int REC = D + 4;
constexpr unsigned SLOT_BYTES = 1024, SLOT_FLAG = 544, SLOT_EPOCH = 576, SLOT_USED = 640;
constexpr int L_PROG = (NW + 3) * REC, L_FLAG = L_PROG + 1, L_SEND = L_PROG + 2, L_RNG = L_PROG + 4, L_FP = L_RNG + 4 * NS,
              L_FTAB = L_FP + 4, L_QF = L_FTAB + NBPS, L_SC = L_QF + 64, L_END = L_SC + NW * 64 * PAGE;

using bf16x2 = __attribute__((ext_vector_type(2))) __bf16;
using u32x4 = __attribute__((ext_vector_type(4))) unsigned;
using i32x4 = __attribute__((ext_vector_type(4))) int;
using f32x2 = __attribute__((ext_vector_type(2))) float;
using f32x4 = __attribute__((ext_vector_type(4))) float;
typedef unsigned long long u64;
#define RLX_AGENT __ATOMIC_RELAXED, __HIP_MEMORY_SCOPE_AGENT
#define RLX_WG __ATOMIC_RELAXED, __HIP_MEMORY_SCOPE_WORKGROUP
#define SB() __builtin_amdgcn_sched_barrier(0)

__device__ __forceinline__ float dot2(unsigned k, unsigned q, float acc) {
  return __builtin_amdgcn_fdot2_f32_bf16(__builtin_bit_cast(bf16x2, k), __builtin_bit_cast(bf16x2, q), acc, false);
}
template <int CTRL> __device__ __forceinline__ float dpp(float x) {
  return __builtin_bit_cast(float, __builtin_amdgcn_mov_dpp(__builtin_bit_cast(int, x), CTRL, 0xf, 0xf, true));
}
template <int CTRL> __device__ __forceinline__ int dppi(int x) { return __builtin_amdgcn_mov_dpp(x, CTRL, 0xf, 0xf, true); }
__device__ __forceinline__ float row_sum(float x) {
  x += dpp<0x128>(x);
  x += dpp<0x124>(x);
  x += dpp<0x122>(x);
  x += dpp<0x121>(x);
  return x;
}
__device__ __forceinline__ float xrow_max(float x) {
  auto s = __builtin_amdgcn_permlane16_swap(__float_as_uint(x), __float_as_uint(x), false, false);
  x = fmaxf(__uint_as_float(s[0]), __uint_as_float(s[1]));
  auto t = __builtin_amdgcn_permlane32_swap(__float_as_uint(x), __float_as_uint(x), false, false);
  return fmaxf(__uint_as_float(t[0]), __uint_as_float(t[1]));
}
__device__ __forceinline__ float xrow_sum(float x) {
  auto s = __builtin_amdgcn_permlane16_swap(__float_as_uint(x), __float_as_uint(x), false, false);
  x = __uint_as_float(s[0]) + __uint_as_float(s[1]);
  auto t = __builtin_amdgcn_permlane32_swap(__float_as_uint(x), __float_as_uint(x), false, false);
  return __uint_as_float(t[0]) + __uint_as_float(t[1]);
}
__device__ __forceinline__ int rfl(int x) { return __builtin_amdgcn_readfirstlane(x); }

__global__ void __launch_bounds__(NW * 64, 1)
paged_attn_kernel(const unsigned short* __restrict__ q, const unsigned short* __restrict__ kc,
                  const unsigned short* __restrict__ vc, const int* __restrict__ bt,
                  const int* __restrict__ lens, unsigned short* __restrict__ out, unsigned char* ws, unsigned kv_bytes, float page_ticks) {
  __shared__ __attribute__((aligned(16))) int lds[L_END];
  unsigned cpf;
  {
    u64 pc; unsigned off = (threadIdx.x & 63u) * 128u;
    asm volatile("s_getpc_b64 %0\n.Lcpf_here:\n\tv_min_u32 %1, .Lcpf_end-.Lcpf_here, %1" : "=s"(pc), "+v"(off));
    cpf = *reinterpret_cast<const __attribute__((address_space(1))) unsigned*>(pc + off);
    asm volatile("" ::: "memory");
  }
  const int bh = blockIdx.x, b = bh >> 5, h = bh & 31;
  const int tid = threadIdx.x, lane = tid & 63, r = lane >> 4, c = lane & 15;
  const int wave = rfl(tid >> 6);
  constexpr float C2 = 0.088388347648318440f * 1.4426950408889634f;

  asm volatile("" : "+s"(kv_bytes), "+s"(page_ticks) : "s"(vc), "s"(out), "s"(ws));
  int len = lens[b];
  const int phys0 = bt[b * NBPS + wave], phys1 = bt[b * NBPS + NS + wave];
  SB();
  const unsigned wsl = __hip_atomic_load(reinterpret_cast<const unsigned*>(ws + (size_t)bh * SLOT_BYTES + min(lane * 64u, SLOT_USED - 64u)), RLX_AGENT);
  const int bt0 = bt[b * NBPS + lane], bt1 = bt[b * NBPS + 64 + lane];
  const u32x4 qown = __builtin_nontemporal_load(reinterpret_cast<const u32x4*>(q + (size_t)bh * D + 8 * c));
  int lv = lens[lane & (B - 1)];
  if (tid == 0) *reinterpret_cast<i32x4*>(lds + L_PROG) = i32x4{0, 0, 0, 0};
  unsigned vb[4]; int rt[4];
#pragma unroll
  for (int t = 0; t < 4; ++t) {
    rt[t] = r + 4 * t; vb[t] = (unsigned)rt[t] * TOK_BYTES + (unsigned)c * 16u;
    asm volatile("" : "+v"(vb[t]), "+v"(rt[t]));
  }
  SB();
  const bool allm = len <= 0;
  len = allm ? SKV : min(len, SKV);
  u32x4 Ab[4], Bb[4];
#define SLOT(phys_, h_, rem_) ((unsigned)(phys_) * PAGE_BYTES + (unsigned)(h_) * (D * 2) + (unsigned)max(0, min((rem_), PAGE)))
#define RSRC(base_, t_) __builtin_amdgcn_make_buffer_rsrc((void*)(base_), 0, min(((t_) & ~(PAGE_BYTES - 1u)) + ((t_) & 31u) * TOK_BYTES, kv_bytes), 0x00020000)
#define ISSUE(X, rs, so_) do { _Pragma("unroll") for (int t_ = 0; t_ < 4; ++t_) X[t_] = __builtin_amdgcn_raw_buffer_load_b128(rs, vb[t_], so_, AUX_NT); } while (0)
  unsigned tA = SLOT(phys0, h, wave < NS ? len - wave * PAGE : 0), tB = SLOT(phys1, h, wave < NS ? len - (NS + wave) * PAGE : 0);
  ISSUE(Ab, RSRC(kc, tA), tA & ~255u); SB();
  ISSUE(Bb, RSRC(kc, tB), tB & ~255u); SB();
  const unsigned t_ref = (unsigned)__builtin_amdgcn_s_memrealtime();
  asm volatile("s_waitcnt lgkmcnt(0)\n\ts_barrier" ::: "memory");

  const int np = (len + PAGE - 1) / PAGE;
  const int d0 = 8 * c + 2 * r;
  int pnext = 0, pend = 0;
  int give = 0, F = 0;
  int dbh = 0, f1n = 0, g1n = 0, n0 = 0;
  u64 rtag = 0, ftag = 0;
  if (wave == NS) {
    const int sj = lane & (B - 1);
    rtag = __hip_atomic_load(reinterpret_cast<u64*>(ws + (size_t)bh * SLOT_BYTES + SLOT_EPOCH), RLX_AGENT);
    const u64 allmask = __ballot(lv <= 0);
    lv = lv <= 0 ? SKV : min(lv, SKV);
    const int npv = (lv + PAGE - 1) / PAGE;
    const int key = lv * B + (B - 1 - sj);
    int rank = 0;
#pragma unroll
    for (int j = 0; j < B; ++j) rank += __builtin_amdgcn_readlane(key, j) > key ? 1 : 0;
    int tot = npv + dppi<0x124>(npv);
    tot += dppi<0x122>(tot);
    tot += dppi<0x121>(tot);
    const int pos = rank < B / 2 ? B - 2 - 2 * rank : 2 * rank - B + 1;
    const int mypos = __builtin_amdgcn_readlane(pos, b);
    const int pb = __builtin_ctzll(__ballot(pos == ((mypos + B - 1) & (B - 1))));
    dbh = pb * H + (h ^ HX);
    const int f0 = bt[pb * NBPS + lane], f1 = bt[pb * NBPS + 64 + lane];
    int qf = __builtin_nontemporal_load(reinterpret_cast<const int*>(q) + dbh * (D / 2) + lane);
    ftag = __hip_atomic_load(reinterpret_cast<u64*>(ws + (size_t)dbh * SLOT_BYTES + SLOT_EPOCH), RLX_AGENT);
    const int ex = npv - (tot / B + (pos < tot % B ? 1 : 0));
    const int pk = (ex << 3) | pos;
    int pre = 0;
#pragma unroll
    for (int j = 0; j < B; ++j) { const int pj = __builtin_amdgcn_readlane(pk, j); pre += (pj & 7) <= pos ? pj >> 3 : 0; }
    int pmin = min(pre, dppi<0x124>(pre));
    pmin = min(pmin, dppi<0x122>(pmin));
    pmin = min(pmin, dppi<0x121>(pmin));
    const int gv = max(0, min(min(pre - pmin, tot * FMAXN / (32 * B * NS)), npv - K0 * NS));
    give = __builtin_amdgcn_readlane(gv, b);
    F = __builtin_amdgcn_readlane(gv, pb);
    const int lo = min(np, K0 * NS), own = np - give - lo;
    const int top = max(0, min(own + F + lo - (rfl(tot) + B * NS * 2 - 1) / (B * NS * 2) * (NS * 2), (own + F + lo) / NW));
    const int S = min(top, own), rest = own - S;
    const int pl = __builtin_amdgcn_readlane(lv, pb), pnp = (pl + PAGE - 1) / PAGE;
    f1n = F * (32 * B * NS) > rfl(tot) * FSPLIT ? F / 2 : 0;
    g1n = give * (32 * B * NS) > rfl(tot) * FSPLIT ? give / 2 : 0;
    const int c2 = max(0, min((rest + F + 1) / (2 * NS) * 2 - (F - f1n), rest));
    const int c1 = max(0, min((rest + F + 3) / (2 * NS) * 2 - f1n, rest - c2)), c0 = rest - c1 - c2;
    if (lane == 0) {
      *reinterpret_cast<i32x4*>(lds + L_RNG) = i32x4{lo, lo + c0, 0, 0};
      *reinterpret_cast<i32x4*>(lds + L_RNG + 4) = i32x4{lo + c0, lo + c0 + c1, pnp - F, pnp - F + f1n};
      *reinterpret_cast<i32x4*>(lds + L_RNG + 8) = i32x4{lo + c0 + c1, lo + rest, pnp - F + f1n, pnp};
      lds[L_FP] = pl; lds[L_FP + 1] = tot;
      asm volatile("s_waitcnt lgkmcnt(0)" ::: "memory");
      __hip_atomic_store(lds + L_FLAG, 1, RLX_WG);
    }
    pnext = rfl(lo + rest); pend = rfl(pnext + S); n0 = K0 + c0;
    if ((allmask >> pb) & 1) qf = 0;
    lds[L_FTAB + lane] = f0; lds[L_FTAB + 64 + lane] = f1; lds[L_QF + lane] = qf;
    asm volatile("s_waitcnt lgkmcnt(0)" ::: "memory");
    if (lane == 0) __hip_atomic_store(lds + L_FLAG, 2, RLX_WG);
    __builtin_amdgcn_s_waitcnt(0x0F70);
  }

  float M = -1e30f, L = 0.f, a0 = 0.f, a1 = 0.f;
  float* ldsf = reinterpret_cast<float*>(lds);
  if (wave < NS || pnext < pend) {
    u32x4 qv = allm ? u32x4{0u, 0u, 0u, 0u} : qown;
    float m = -1e30f, mo = -1e30f, l = 0.f;
    f32x2 acc2[4];
#pragma unroll
    for (int k = 0; k < 4; ++k) acc2[k] = f32x2{0.f, 0.f};
    if (wave >= NS)
      while (rfl(__hip_atomic_load(lds + L_PROG, RLX_WG)) < n0 - (pend - pnext) - 2) __builtin_amdgcn_s_sleep(8);
    int tau = 0;
    const int t0 = rfl((int)(page_ticks * (float)(PACE_T0_NS * PACE_GBS / (2.0 * PAGE_BYTES))));
#define PACE(i_) do { const int due = t0 + (((NS * (i_) + wave) * tau) >> 8); \
      while ((int)((unsigned)__builtin_amdgcn_s_memrealtime() - t_ref) < due) __builtin_amdgcn_s_sleep(1); } while (0)
    int fst = 0, cf = 0, no = 64, n = 64, sfill = -1;
    unsigned tK = SLOT(__builtin_amdgcn_ds_bpermute((lane * NS + wave) << 2, bt0), h, lane < K0 && wave < NS ? len - (lane * NS + wave) * PAGE : 0), tV = 0;
    f32x4* sc = reinterpret_cast<f32x4*>(ldsf + L_SC + wave * 64 * PAGE) + r;
#define KSTEP(X, tX, s_) do { \
      const unsigned tn = (s_) + 2 < n ? __builtin_amdgcn_readlane(tK, (s_) + 2) : __builtin_amdgcn_readlane(tV, (s_) + 2 - n); \
      const __amdgpu_buffer_rsrc_t nrs = RSRC((s_) + 2 < n ? kc : vc, tn); \
      if ((s_) == no) { mo = m; m = -1e30f; qv = *reinterpret_cast<const u32x4*>(lds + L_QF + 4 * c); } \
      SB(); \
      float x[4]; \
      _Pragma("unroll") for (int t = 0; t < 4; ++t) { \
        float d = 0.f; \
        _Pragma("unroll") for (int k = 0; k < 4; ++k) d = dot2(X[t][k], qv[k], d); \
        x[t] = d; \
        asm volatile("" : "+v"(x[t]) :: "memory"); \
        SB(); \
        X[t] = __builtin_amdgcn_raw_buffer_load_b128(nrs, vb[t], tn & ~255u, AUX_NT); \
        asm volatile("" ::: "memory"); \
        SB(); \
      } \
      _Pragma("unroll") for (int t = 0; t < 4; ++t) { \
        x[t] = row_sum(x[t]) * C2; \
        x[t] = (rt[t] < (int)(tX & 31u)) ? x[t] : -INFINITY; \
        m = fmaxf(m, x[t]); \
      } \
      if (c == 0) sc[4 * (s_)] = f32x4{x[0], x[1], x[2], x[3]}; \
      tX = tn; \
    } while (0)
#define VSTEP(X, j_) do { \
      const unsigned tn = __builtin_amdgcn_readlane(tV, (j_) + 2); \
      const __amdgpu_buffer_rsrc_t nrs = RSRC(vc, tn); \
      const f32x4 x = sc[4 * ((j_) < cf ? no + (j_) : (j_) - cf)]; \
      float p[4]; \
      _Pragma("unroll") for (int t = 0; t < 4; ++t) p[t] = __builtin_amdgcn_exp2f(x[t] - m); \
      l += (p[0] + p[1]) + (p[2] + p[3]); \
      SB(); \
      _Pragma("unroll") for (int t = 0; t < 4; ++t) { \
        _Pragma("unroll") for (int k = 0; k < 4; ++k) { \
          const unsigned w = X[t][k]; \
          const f32x2 v2 = {__builtin_bit_cast(float, w << 16), __builtin_bit_cast(float, w & 0xffff0000u)}; \
          acc2[k] = __builtin_elementwise_fma(f32x2{p[t], p[t]}, v2, acc2[k]); \
        } \
        asm volatile("" : "+v"(acc2[0]), "+v"(acc2[1]), "+v"(acc2[2]), "+v"(acc2[3]) :: "memory"); \
        SB(); \
        X[t] = __builtin_amdgcn_raw_buffer_load_b128(nrs, vb[t], tn & ~255u, AUX_NT); \
        asm volatile("" ::: "memory"); \
        SB(); \
      } \
    } while (0)
#define FLUSH() do { \
      M = m; \
      L = xrow_sum(l); \
      float s4[4]; \
      _Pragma("unroll") for (int k = 0; k < 4; ++k) {       \
        auto w_ = __builtin_amdgcn_permlane32_swap(__float_as_uint(acc2[k >> 1][k & 1]), __float_as_uint(acc2[2 + (k >> 1)][k & 1]), false, false); \
        s4[k] = __uint_as_float(w_[0]) + __uint_as_float(w_[1]); } \
      auto u0_ = __builtin_amdgcn_permlane16_swap(__float_as_uint(s4[0]), __float_as_uint(s4[2]), false, false); \
      auto u1_ = __builtin_amdgcn_permlane16_swap(__float_as_uint(s4[1]), __float_as_uint(s4[3]), false, false); \
      a0 = __uint_as_float(u0_[0]) + __uint_as_float(u0_[1]); \
      a1 = __uint_as_float(u1_[0]) + __uint_as_float(u1_[1]); } while (0)
#define PUBLISH() do { \
      float* srec = ldsf + (NW + wave) * REC; \
      *reinterpret_cast<f32x2*>(srec + 4 + d0) = f32x2{a0, a1}; \
      if (lane == 0) *reinterpret_cast<f32x2*>(srec) = f32x2{M, L}; \
      asm volatile("s_waitcnt lgkmcnt(0)" ::: "memory"); \
      if (lane == 0) __hip_atomic_store(lds + L_SEND + wave - 1, 1, RLX_WG); \
      m = mo; l = 0.f; \
      _Pragma("unroll") for (int k = 0; k < 4; ++k) acc2[k] = f32x2{0.f, 0.f}; } while (0)
    for (int s = 0; s < n; s += 2) {
      s = rfl(s);
      if (tid == 0) __hip_atomic_store(lds + L_PROG, s, RLX_WG);
      if (s == K0 - 2) {
        if (wave < NS) {
          while (rfl(__hip_atomic_load(lds + L_FLAG, RLX_WG)) < 1) __builtin_amdgcn_s_sleep(1);
          asm volatile("" ::: "memory");
          const i32x4 rg = *reinterpret_cast<const i32x4*>(lds + L_RNG + 4 * wave);
          pnext = rfl(rg[0]); pend = rfl(rg[1]); fst = rfl(rg[2]); cf = rfl(rg[3]) - fst;
        }
        no = K0 + pend - pnext + ((pend - pnext + cf) & 1); n = no + cf;
        sfill = no >= K0 + 2 ? K0 : K0 - 2;
        if (wave < NS) tau = rfl((int)((float)lds[L_FP + 1] * page_ticks * (256.f / (2 * NS)) * __builtin_amdgcn_rcpf((float)n)));
        const int pn = pnext + lane - K0;
        const int p0 = __builtin_amdgcn_ds_bpermute(pn << 2, bt0), p1 = __builtin_amdgcn_ds_bpermute(pn << 2, bt1);
        if (lane >= K0) tK = pn < pend ? SLOT((pn & 64) ? p1 : p0, h, len - pn * PAGE) : 0u;
      }
      if (s == sfill) {
        if (cf > 0) {
          while (rfl(__hip_atomic_load(lds + L_FLAG, RLX_WG)) < 2) __builtin_amdgcn_s_sleep(1);
          asm volatile("" ::: "memory");
          const int pn = fst + lane - no;
          if (lane >= no && lane < n) tK = SLOT(lds[L_FTAB + pn], h ^ HX, lds[L_FP] - pn * PAGE);
        }
        tV = __builtin_amdgcn_ds_bpermute((lane < cf ? no + lane : lane < n ? lane - cf : 63) << 2, tK);
      }
      PACE(s);
      KSTEP(Ab, tA, s);
      PACE(s + 1);
      KSTEP(Bb, tB, s + 1);
    }
    m = xrow_max(m); mo = xrow_max(mo);
    for (int j = 0;; j += 2) {
      j = rfl(j);
      if (tid == 0) __hip_atomic_store(lds + L_PROG, n + j, RLX_WG);
      if (j + 2 < n) PACE(n + j);
      VSTEP(Ab, j);
      if (__builtin_expect(j + 1 == cf, 0)) { FLUSH(); PUBLISH(); }
      if (j + 2 < n) PACE(n + j + 1);
      VSTEP(Bb, j + 1);
      const bool fin = j + 2 == n;
      if (j + 2 == cf || fin) {
        FLUSH();
        if (fin) break;
        PUBLISH();
      }
    }
    if (tid == 0) __hip_atomic_store(lds + L_PROG, 1 << 20, RLX_WG);
  }
  if (wave == NS) {
    const __amdgpu_buffer_rsrc_t wrs = __builtin_amdgcn_make_buffer_rsrc(ws, 0, B * H * SLOT_BYTES, 0x00020000);
    const unsigned loff = lane < REC / 4 ? lane * 16u : 0x80000000u;
    u32x4* lrec = reinterpret_cast<u32x4*>(lds + NW * REC) + min(lane, REC / 4 - 1);
    if (give <= 0) *lrec = u32x4{lane ? 0u : __float_as_uint(-1e30f), 0u, 0u, 0u};
    bool send = F > 0, recv = give > 0;
    for (unsigned spins = 0; send || recv; ++spins) {
      const int prog = rfl(__hip_atomic_load(lds + L_PROG, RLX_WG));
      const int s1 = rfl(__hip_atomic_load(lds + L_SEND, RLX_WG)), s2 = rfl(__hip_atomic_load(lds + L_SEND + 1, RLX_WG));
      if (send && s2 && (s1 || f1n == 0)) {
        asm volatile("" ::: "memory");
        f32x4 rec2 = *reinterpret_cast<const f32x4*>(lrec + 2 * (REC / 4));
        if (f1n) {
          const f32x4 rec1 = *reinterpret_cast<const f32x4*>(lrec + REC / 4);
          const float m1 = ldsf[(NW + 1) * REC], m2 = ldsf[(NW + 2) * REC], mm = fmaxf(m1, m2);
          const float e1 = __builtin_amdgcn_exp2f(m1 - mm), e2 = __builtin_amdgcn_exp2f(m2 - mm);
          rec2 = rec1 * e1 + rec2 * e2;
          if (lane == 0) rec2[0] = mm;
        }
        __builtin_amdgcn_raw_buffer_store_b128(__builtin_bit_cast(u32x4, rec2), wrs, loff, (unsigned)rfl(dbh) * SLOT_BYTES, AUX_SC1);
        asm volatile("s_waitcnt vmcnt(0)" ::: "memory");
        if (lane == 0) __hip_atomic_store(reinterpret_cast<u64*>(ws + (size_t)dbh * SLOT_BYTES + SLOT_FLAG), ftag + 1u, RLX_AGENT);
        send = false;
      }
      if (recv && prog >= n0 + give - g1n + PWAIT) {
        const u64 fl = __hip_atomic_load(reinterpret_cast<u64*>(ws + (size_t)bh * SLOT_BYTES + SLOT_FLAG), RLX_AGENT);
        if (__all(fl == rtag + 1u)) {
          __builtin_amdgcn_fence(__ATOMIC_ACQUIRE, "agent");
          asm volatile("" ::: "memory");
          const u32x4 pay = __builtin_amdgcn_raw_buffer_load_b128(wrs, loff, (unsigned)bh * SLOT_BYTES, AUX_SC1);
          if (lane < REC / 4) *lrec = pay;
          asm volatile("s_waitcnt vmcnt(0)" ::: "memory");
          if (lane == 0) __hip_atomic_store(reinterpret_cast<u64*>(ws + (size_t)bh * SLOT_BYTES + SLOT_EPOCH), rtag + 1u, RLX_AGENT);
          recv = false;
        } else if (spins > (1u << 23)) {
          *lrec = u32x4{0u, 0x7fc00000u, 0u, 0u};
          recv = false;
        }
      }
      __builtin_amdgcn_s_sleep(6);
    }
  }
  float* rec = ldsf + wave * REC;
  *reinterpret_cast<f32x2*>(rec + 4 + d0) = f32x2{a0, a1};
  if (lane == 0) *reinterpret_cast<f32x2*>(rec) = f32x2{M, L};
  asm volatile("" :: "v"(cpf), "v"(wsl));
  __syncthreads();
  if (tid < 64) {
    float mw[NW + 1], lw[NW + 1], MM = -1e30f;
#pragma unroll
    for (int w = 0; w <= NW; ++w) {
      const f32x2 ml = *reinterpret_cast<const f32x2*>(ldsf + w * REC);
      mw[w] = ml[0]; lw[w] = ml[1];
      MM = fmaxf(MM, mw[w]);
    }
    float LL = 0.f, o0 = 0.f, o1 = 0.f;
#pragma unroll
    for (int w = 0; w <= NW; ++w) {
      const float s = __builtin_amdgcn_exp2f(mw[w] - MM);
      const f32x2 a = *reinterpret_cast<const f32x2*>(ldsf + w * REC + 4 + 2 * tid);
      LL = fmaf(s, lw[w], LL); o0 = fmaf(s, a[0], o0); o1 = fmaf(s, a[1], o1);
    }
    const float inv = __builtin_amdgcn_rcpf(LL);
    const __hip_bfloat16 r0 = __float2bfloat16(o0 * inv), r1 = __float2bfloat16(o1 * inv);
    const unsigned pk = (unsigned)__builtin_bit_cast(unsigned short, r0) | ((unsigned)__builtin_bit_cast(unsigned short, r1) << 16);
    asm volatile(".Lcpf_end:");
    *reinterpret_cast<unsigned*>(out + (size_t)bh * D + 2 * tid) = pk;
  }
}
}

extern "C" void kernel_launch(void* const* d_in, const int* in_sizes, int n_in, void* d_out, int out_size,
                              void* d_ws, size_t ws_size, hipStream_t stream) {
  const unsigned kv_bytes = (unsigned)in_sizes[1] * 2u;
  int dev = 0, khz = 0;
  if (hipGetDevice(&dev) != hipSuccess || hipDeviceGetAttribute(&khz, hipDeviceAttributeWallClockRate, dev) != hipSuccess) khz = 0;
  const float page_ticks = (float)(2.0 * PAGE_BYTES / PACE_GBS * 1e-6 * khz);
  paged_attn_kernel<<<B * H, NW * 64, 0, stream>>>(
      (const unsigned short*)d_in[0], (const unsigned short*)d_in[1], (const unsigned short*)d_in[2],
      (const int*)d_in[3], (const int*)d_in[4], (unsigned short*)d_out, (unsigned char*)d_ws, kv_bytes, page_ticks);
}
```

```cpp
#include <hip/hip_runtime.h>
#include <hip/hip_bf16.h>
#include <stdint.h>

namespace {
constexpr int B = 8, H = 32, D = 128, PAGE = 16, NBPS = 128, SKV = NBPS * PAGE;
constexpr int NS = 3, NW = NS + 1;
constexpr int K0 = 4;
constexpr int HX = 2;
constexpr int FMAXN = 48, FSPLIT = 12;
constexpr int PWAIT = 6;
constexpr double PACE_GBS = 7780;
constexpr double PACE_T0_NS = 500;
constexpr int AUX_NT = 2, AUX_SC1 = 16;
constexpr unsigned TOK_BYTES = H * D * 2;
constexpr unsigned PAGE_BYTES = PAGE * TOK_BYTES;
constexpr int REC = D + 4;
constexpr unsigned SLOT_BYTES = 1024, SLOT_FLAG = 544, SLOT_EPOCH = 576, SLOT_USED = 640;
constexpr int L_PROG = (NW + 3) * REC, L_FLAG = L_PROG + 1, L_SEND = L_PROG + 2, L_RNG = L_PROG + 4, L_FP = L_RNG + 4 * NS,
              L_FTAB = L_FP + 4, L_QF = L_FTAB + NBPS, L_SC = L_QF + 64, L_END = L_SC + NW * 64 * PAGE;

using bf16x2 = __attribute__((ext_vector_type(2))) __bf16;
using u32x4 = __attribute__((ext_vector_type(4))) unsigned;
using i32x4 = __attribute__((ext_vector_type(4))) int;
using f32x2 = __attribute__((ext_vector_type(2))) float;
using f32x4 = __attribute__((ext_vector_type(4))) float;
typedef unsigned long long u64;
#define RLX_AGENT __ATOMIC_RELAXED, __HIP_MEMORY_SCOPE_AGENT
#define RLX_WG __ATOMIC_RELAXED, __HIP_MEMORY_SCOPE_WORKGROUP
#define SB() __builtin_amdgcn_sched_barrier(0)

__device__ __forceinline__ float dot2(unsigned k, unsigned q, float acc) {
  return __builtin_amdgcn_fdot2_f32_bf16(__builtin_bit_cast(bf16x2, k), __builtin_bit_cast(bf16x2, q), acc, false);
}
template <int CTRL> __device__ __forceinline__ float dpp(float x) {
  return __builtin_bit_cast(float, __builtin_amdgcn_mov_dpp(__builtin_bit_cast(int, x), CTRL, 0xf, 0xf, true));
}
template <int CTRL> __device__ __forceinline__ int dppi(int x) { return __builtin_amdgcn_mov_dpp(x, CTRL, 0xf, 0xf, true); }
__device__ __forceinline__ float row_sum(float x) {
  x += dpp<0x128>(x);
  x += dpp<0x124>(x);
  x += dpp<0x122>(x);
  x += dpp<0x121>(x);
  return x;
}
__device__ __forceinline__ float xrow_max(float x) {
  auto s = __builtin_amdgcn_permlane16_swap(__float_as_uint(x), __float_as_uint(x), false, false);
  x = fmaxf(__uint_as_float(s[0]), __uint_as_float(s[1]));
  auto t = __builtin_amdgcn_permlane32_swap(__float_as_uint(x), __float_as_uint(x), false, false);
  return fmaxf(__uint_as_float(t[0]), __uint_as_float(t[1]));
}
__device__ __forceinline__ float xrow_sum(float x) {
  auto s = __builtin_amdgcn_permlane16_swap(__float_as_uint(x), __float_as_uint(x), false, false);
  x = __uint_as_float(s[0]) + __uint_as_float(s[1]);
  auto t = __builtin_amdgcn_permlane32_swap(__float_as_uint(x), __float_as_uint(x), false, false);
  return __uint_as_float(t[0]) + __uint_as_float(t[1]);
}
__device__ __forceinline__ int rfl(int x) { return __builtin_amdgcn_readfirstlane(x); }

__global__ void __launch_bounds__(NW * 64, 1)
paged_attn_kernel(const unsigned short* __restrict__ q, const unsigned short* __restrict__ kc,
                  const unsigned short* __restrict__ vc, const int* __restrict__ bt,
                  const int* __restrict__ lens, unsigned short* __restrict__ out, unsigned char* ws, unsigned kv_bytes, float page_ticks) {
  __shared__ __attribute__((aligned(16))) int lds[L_END];
  unsigned cpf;
  {
    u64 pc; unsigned off = (threadIdx.x & 63u) * 128u;
    asm volatile("s_getpc_b64 %0\n.Lcpf_here:\n\tv_min_u32 %1, .Lcpf_end-.Lcpf_here, %1" : "=s"(pc), "+v"(off));
    cpf = *reinterpret_cast<const __attribute__((address_space(1))) unsigned*>(pc + off);
    asm volatile("" ::: "memory");
  }
  const int bh = blockIdx.x, b = bh >> 5, h = bh & 31;
  const int tid = threadIdx.x, lane = tid & 63, r = lane >> 4, c = lane & 15;
  const int wave = rfl(tid >> 6);
  constexpr float C2 = 0.088388347648318440f * 1.4426950408889634f;

  asm volatile("" : "+s"(kv_bytes), "+s"(page_ticks) : "s"(vc), "s"(out), "s"(ws));
  int len = lens[b];
  const int phys0 = bt[b * NBPS + wave], phys1 = bt[b * NBPS + NS + wave];
  SB();
  const unsigned wsl = __hip_atomic_load(reinterpret_cast<const unsigned*>(ws + (size_t)bh * SLOT_BYTES + min(lane * 64u, SLOT_USED - 64u)), RLX_AGENT);
  const int bt0 = bt[b * NBPS + lane], bt1 = bt[b * NBPS + 64 + lane];
  const u32x4 qown = __builtin_nontemporal_load(reinterpret_cast<const u32x4*>(q + (size_t)bh * D + 8 * c));
  int lv = lens[lane & (B - 1)];
  if (tid == 0) *reinterpret_cast<i32x4*>(lds + L_PROG) = i32x4{0, 0, 0, 0};
  unsigned vb[4]; int rt[4];
#pragma unroll
  for (int t = 0; t < 4; ++t) {
    rt[t] = r + 4 * t; vb[t] = (unsigned)rt[t] * TOK_BYTES + (unsigned)c * 16u;
    asm volatile("" : "+v"(vb[t]), "+v"(rt[t]));
  }
  SB();
  const bool allm = len <= 0;
  len = allm ? SKV : min(len, SKV);
  u32x4 Ab[4], Bb[4];
#define SLOT(phys_, h_, rem_) ((unsigned)(phys_) * PAGE_BYTES + (unsigned)(h_) * (D * 2) + (unsigned)max(0, min((rem_), PAGE)))
#define RSRC(base_, t_) __builtin_amdgcn_make_buffer_rsrc((void*)(base_), 0, min(((t_) & ~(PAGE_BYTES - 1u)) + ((t_) & 31u) * TOK_BYTES, kv_bytes), 0x00020000)
#define ISSUE(X, rs, so_) do { _Pragma("unroll") for (int t_ = 0; t_ < 4; ++t_) X[t_] = __builtin_amdgcn_raw_buffer_load_b128(rs, vb[t_], so_, AUX_NT); } while (0)
  unsigned tA = SLOT(phys0, h, wave < NS ? len - wave * PAGE : 0), tB = SLOT(phys1, h, wave < NS ? len - (NS + wave) * PAGE : 0);
  ISSUE(Ab, RSRC(kc, tA), tA & ~255u); SB();
  ISSUE(Bb, RSRC(kc, tB), tB & ~255u); SB();
  const unsigned t_ref = (unsigned)__builtin_amdgcn_s_memrealtime();
  asm volatile("s_waitcnt lgkmcnt(0)\n\ts_barrier" ::: "memory");

  const int np = (len + PAGE - 1) / PAGE;
  const int d0 = 8 * c + 2 * r;
  int pnext = 0, pend = 0;
  int give = 0, F = 0;
  int dbh = 0, f1n = 0, g1n = 0, n0 = 0;
  u64 rtag = 0, ftag = 0;
  if (wave == NS) {
    const int sj = lane & (B - 1);
    rtag = __hip_atomic_load(reinterpret_cast<u64*>(ws + (size_t)bh * SLOT_BYTES + SLOT_EPOCH), RLX_AGENT);
    const u64 allmask = __ballot(lv <= 0);
    lv = lv <= 0 ? SKV : min(lv, SKV);
    const int npv = (lv + PAGE - 1) / PAGE;
    const int key = lv * B + (B - 1 - sj);
    int rank = 0;
#pragma unroll
    for (int j = 0; j < B; ++j) rank += __builtin_amdgcn_readlane(key, j) > key ? 1 : 0;
    int tot = npv + dppi<0x124>(npv);
    tot += dppi<0x122>(tot);
    tot += dppi<0x121>(tot);
    const int pos = rank < B / 2 ? B - 2 - 2 * rank : 2 * rank - B + 1;
    const int mypos = __builtin_amdgcn_readlane(pos, b);
    const int pb = __builtin_ctzll(__ballot(pos == ((mypos + B - 1) & (B - 1))));
    dbh = pb * H + (h ^ HX);
    const int f0 = bt[pb * NBPS + lane], f1 = bt[pb * NBPS + 64 + lane];
    int qf = __builtin_nontemporal_load(reinterpret_cast<const int*>(q) + dbh * (D / 2) + lane);
    ftag = __hip_atomic_load(reinterpret_cast<u64*>(ws + (size_t)dbh * SLOT_BYTES + SLOT_EPOCH), RLX_AGENT);
    const int ex = npv - (tot / B + (pos < tot % B ? 1 : 0));
    const int pk = (ex << 3) | pos;
    int pre = 0;
#pragma unroll
    for (int j = 0; j < B; ++j) { const int pj = __builtin_amdgcn_readlane(pk, j); pre += (pj & 7) <= pos ? pj >> 3 : 0; }
    int pmin = min(pre, dppi<0x124>(pre));
    pmin = min(pmin, dppi<0x122>(pmin));
    pmin = min(pmin, dppi<0x121>(pmin));
    const int gv = max(0, min(min(pre - pmin, tot * FMAXN / (32 * B * NS)), npv - K0 * NS));
    give = __builtin_amdgcn_readlane(gv, b);
    F = __builtin_amdgcn_readlane(gv, pb);
    const int lo = min(np, K0 * NS), own = np - give - lo;
    const int top = max(0, min(own + F + lo - (rfl(tot) + B * NS * 2 - 1) / (B * NS * 2) * (NS * 2), (own + F + lo) / NW));
    const int S = min(top, own), rest = own - S;
    const int pl = __builtin_amdgcn_readlane(lv, pb), pnp = (pl + PAGE - 1) / PAGE;
    f1n = F * (32 * B * NS) > rfl(tot) * FSPLIT ? F / 2 : 0;
    g1n = give * (32 * B * NS) > rfl(tot) * FSPLIT ? give / 2 : 0;
    const int c2 = max(0, min((rest + F + 1) / (2 * NS) * 2 - (F - f1n), rest));
    const int c1 = max(0, min((rest + F + 3) / (2 * NS) * 2 - f1n, rest - c2)), c0 = rest - c1 - c2;
    if (lane == 0) {
      *reinterpret_cast<i32x4*>(lds + L_RNG) = i32x4{lo, lo + c0, 0, 0};
      *reinterpret_cast<i32x4*>(lds + L_RNG + 4) = i32x4{lo + c0, lo + c0 + c1, pnp - F, pnp - F + f1n};
      *reinterpret_cast<i32x4*>(lds + L_RNG + 8) = i32x4{lo + c0 + c1, lo + rest, pnp - F + f1n, pnp};
      lds[L_FP] = pl; lds[L_FP + 1] = tot;
      asm volatile("s_waitcnt lgkmcnt(0)" ::: "memory");
      __hip_atomic_store(lds + L_FLAG, 1, RLX_WG);
    }
    pnext = rfl(lo + rest); pend = rfl(pnext + S); n0 = K0 + c0;
    if ((allmask >> pb) & 1) qf = 0;
    lds[L_FTAB + lane] = f0; lds[L_FTAB + 64 + lane] = f1; lds[L_QF + lane] = qf;
    asm volatile("s_waitcnt lgkmcnt(0)" ::: "memory");
    if (lane == 0) __hip_atomic_store(lds + L_FLAG, 2, RLX_WG);
    __builtin_amdgcn_s_waitcnt(0x0F70);
  }

  float M = -1e30f, L = 0.f, a0 = 0.f, a1 = 0.f;
  float* ldsf = reinterpret_cast<float*>(lds);
  if (wave < NS || pnext < pend) {
    u32x4 qv = allm ? u32x4{0u, 0u, 0u, 0u} : qown;
    float m = -1e30f, mo = -1e30f, l = 0.f;
    f32x2 acc2[4];
#pragma unroll
    for (int k = 0; k < 4; ++k) acc2[k] = f32x2{0.f, 0.f};
    if (wave >= NS)
      while (rfl(__hip_atomic_load(lds + L_PROG, RLX_WG)) < n0 - (pend - pnext) - 2) __builtin_amdgcn_s_sleep(8);
    int tau = 0, now = 0;
    const int t0 = rfl((int)(page_ticks * (float)(PACE_T0_NS * PACE_GBS / (2.0 * PAGE_BYTES))));
#define PACE(i_) do { const int due = t0 + (((NS * (i_) + wave) * tau) >> 8); \
      if (now < due) while ((now = (int)((unsigned)__builtin_amdgcn_s_memrealtime() - t_ref)) < due) __builtin_amdgcn_s_sleep(1); } while (0)
    int fst = 0, cf = 0, no = 64, n = 64, sfill = -1;
    unsigned tK = SLOT(__builtin_amdgcn_ds_bpermute((lane * NS + wave) << 2, bt0), h, lane < K0 && wave < NS ? len - (lane * NS + wave) * PAGE : 0), tV = 0;
    f32x4* sc = reinterpret_cast<f32x4*>(ldsf + L_SC + wave * 64 * PAGE) + r;
#define KSTEP(X, tX, s_) do { \
      const unsigned tn = (s_) + 2 < n ? __builtin_amdgcn_readlane(tK, (s_) + 2) : __builtin_amdgcn_readlane(tV, (s_) + 2 - n); \
      const __amdgpu_buffer_rsrc_t nrs = RSRC((s_) + 2 < n ? kc : vc, tn); \
      if ((s_) == no) { mo = m; m = -1e30f; qv = *reinterpret_cast<const u32x4*>(lds + L_QF + 4 * c); } \
      SB(); \
      float x[4]; \
      _Pragma("unroll") for (int t = 0; t < 4; ++t) { \
        float d = 0.f; \
        _Pragma("unroll") for (int k = 0; k < 4; ++k) d = dot2(X[t][k], qv[k], d); \
        x[t] = d; \
        asm volatile("" : "+v"(x[t]) :: "memory"); \
        SB(); \
        X[t] = __builtin_amdgcn_raw_buffer_load_b128(nrs, vb[t], tn & ~255u, AUX_NT); \
        asm volatile("" ::: "memory"); \
        SB(); \
      } \
      _Pragma("unroll") for (int t = 0; t < 4; ++t) { \
        x[t] = row_sum(x[t]) * C2; \
        x[t] = (rt[t] < (int)(tX & 31u)) ? x[t] : -INFINITY; \
        m = fmaxf(m, x[t]); \
      } \
      if (c == 0) sc[4 * (s_)] = f32x4{x[0], x[1], x[2], x[3]}; \
      tX = tn; \
    } while (0)
#define VSTEP(X, j_) do { \
      const unsigned tn = __builtin_amdgcn_readlane(tV, (j_) + 2); \
      const __amdgpu_buffer_rsrc_t nrs = RSRC(vc, tn); \
      const f32x4 x = sc[4 * ((j_) < cf ? no + (j_) : (j_) - cf)]; \
      float p[4]; \
      _Pragma("unroll") for (int t = 0; t < 4; ++t) p[t] = __builtin_amdgcn_exp2f(x[t] - m); \
      l += (p[0] + p[1]) + (p[2] + p[3]); \
      SB(); \
      _Pragma("unroll") for (int t = 0; t < 4; ++t) { \
        _Pragma("unroll") for (int k = 0; k < 4; ++k) { \
          const unsigned w = X[t][k]; \
          const f32x2 v2 = {__builtin_bit_cast(float, w << 16), __builtin_bit_cast(float, w & 0xffff0000u)}; \
          acc2[k] = __builtin_elementwise_fma(f32x2{p[t], p[t]}, v2, acc2[k]); \
        } \
        asm volatile("" : "+v"(acc2[0]), "+v"(acc2[1]), "+v"(acc2[2]), "+v"(acc2[3]) :: "memory"); \
        SB(); \
        X[t] = __builtin_amdgcn_raw_buffer_load_b128(nrs, vb[t], tn & ~255u, AUX_NT); \
        asm volatile("" ::: "memory"); \
        SB(); \
      } \
    } while (0)
#define FLUSH() do { \
      M = m; \
      L = xrow_sum(l); \
      float s4[4]; \
      _Pragma("unroll") for (int k = 0; k < 4; ++k) {       \
        auto w_ = __builtin_amdgcn_permlane32_swap(__float_as_uint(acc2[k >> 1][k & 1]), __float_as_uint(acc2[2 + (k >> 1)][k & 1]), false, false); \
        s4[k] = __uint_as_float(w_[0]) + __uint_as_float(w_[1]); } \
      auto u0_ = __builtin_amdgcn_permlane16_swap(__float_as_uint(s4[0]), __float_as_uint(s4[2]), false, false); \
      auto u1_ = __builtin_amdgcn_permlane16_swap(__float_as_uint(s4[1]), __float_as_uint(s4[3]), false, false); \
      a0 = __uint_as_float(u0_[0]) + __uint_as_float(u0_[1]); \
      a1 = __uint_as_float(u1_[0]) + __uint_as_float(u1_[1]); } while (0)
#define PUBLISH() do { \
      float* srec = ldsf + (NW + wave) * REC; \
      *reinterpret_cast<f32x2*>(srec + 4 + d0) = f32x2{a0, a1}; \
      if (lane == 0) *reinterpret_cast<f32x2*>(srec) = f32x2{M, L}; \
      asm volatile("s_waitcnt lgkmcnt(0)" ::: "memory"); \
      if (lane == 0) __hip_atomic_store(lds + L_SEND + wave - 1, 1, RLX_WG); \
      m = mo; l = 0.f; \
      _Pragma("unroll") for (int k = 0; k < 4; ++k) acc2[k] = f32x2{0.f, 0.f}; } while (0)
    for (int s = 0; s < n; s += 2) {
      s = rfl(s);
      if (tid == 0) __hip_atomic_store(lds + L_PROG, s, RLX_WG);
      if (s == K0 - 2) {
        if (wave < NS) {
          while (rfl(__hip_atomic_load(lds + L_FLAG, RLX_WG)) < 1) __builtin_amdgcn_s_sleep(1);
          asm volatile("" ::: "memory");
          const i32x4 rg = *reinterpret_cast<const i32x4*>(lds + L_RNG + 4 * wave);
          pnext = rfl(rg[0]); pend = rfl(rg[1]); fst = rfl(rg[2]); cf = rfl(rg[3]) - fst;
        }
        no = K0 + pend - pnext + ((pend - pnext + cf) & 1); n = no + cf;
        sfill = no >= K0 + 2 ? K0 : K0 - 2;
        if (wave < NS) tau = rfl((int)((float)lds[L_FP + 1] * page_ticks * (256.f / (2 * NS)) * __builtin_amdgcn_rcpf((float)n)));
        const int pn = pnext + lane - K0;
        const int p0 = __builtin_amdgcn_ds_bpermute(pn << 2, bt0), p1 = __builtin_amdgcn_ds_bpermute(pn << 2, bt1);
        if (lane >= K0) tK = pn < pend ? SLOT((pn & 64) ? p1 : p0, h, len - pn * PAGE) : 0u;
        const bool far = lane >= K0 && tK >= (0x80000000u >> __builtin_clz(kv_bytes));
        const u64 fm = __ballot(far);
        if (fm) {
          const int below = __builtin_amdgcn_mbcnt_hi((unsigned)(fm >> 32), __builtin_amdgcn_mbcnt_lo((unsigned)fm, 0u));
          const int dst = far ? K0 + below : lane < K0 ? lane : lane + __popcll(fm) - below;
          tK = __builtin_amdgcn_ds_permute(dst << 2, tK);
        }
      }
      if (s == sfill) {
        if (cf > 0) {
          while (rfl(__hip_atomic_load(lds + L_FLAG, RLX_WG)) < 2) __builtin_amdgcn_s_sleep(1);
          asm volatile("" ::: "memory");
          const int pn = fst + lane - no;
          if (lane >= no && lane < n) tK = SLOT(lds[L_FTAB + pn], h ^ HX, lds[L_FP] - pn * PAGE);
        }
        tV = __builtin_amdgcn_ds_bpermute((lane < cf ? no + lane : lane < n ? lane - cf : 63) << 2, tK);
      }
      PACE(s);
      KSTEP(Ab, tA, s);
      PACE(s + 1);
      KSTEP(Bb, tB, s + 1);
    }
    m = xrow_max(m); mo = xrow_max(mo);
    for (int j = 0;; j += 2) {
      j = rfl(j);
      if (tid == 0) __hip_atomic_store(lds + L_PROG, n + j, RLX_WG);
      if (j + 2 < n) PACE(n + j);
      VSTEP(Ab, j);
      if (__builtin_expect(j + 1 == cf, 0)) { FLUSH(); PUBLISH(); }
      if (j + 2 < n) PACE(n + j + 1);
      VSTEP(Bb, j + 1);
      const bool fin = j + 2 == n;
      if (j + 2 == cf || fin) {
        FLUSH();
        if (fin) break;
        PUBLISH();
      }
    }
    if (tid == 0) __hip_atomic_store(lds + L_PROG, 1 << 20, RLX_WG);
  }
  if (wave == NS) {
    const __amdgpu_buffer_rsrc_t wrs = __builtin_amdgcn_make_buffer_rsrc(ws, 0, B * H * SLOT_BYTES, 0x00020000);
    const unsigned loff = lane < REC / 4 ? lane * 16u : 0x80000000u;
    u32x4* lrec = reinterpret_cast<u32x4*>(lds + NW * REC) + min(lane, REC / 4 - 1);
    if (give <= 0) *lrec = u32x4{lane ? 0u : __float_as_uint(-1e30f), 0u, 0u, 0u};
    bool send = F > 0, recv = give > 0;
    for (unsigned spins = 0; send || recv; ++spins) {
      const int prog = rfl(__hip_atomic_load(lds + L_PROG, RLX_WG));
      const int s1 = rfl(__hip_atomic_load(lds + L_SEND, RLX_WG)), s2 = rfl(__hip_atomic_load(lds + L_SEND + 1, RLX_WG));
      if (send && s2 && (s1 || f1n == 0)) {
        asm volatile("" ::: "memory");
        f32x4 rec2 = *reinterpret_cast<const f32x4*>(lrec + 2 * (REC / 4));
        if (f1n) {
          const f32x4 rec1 = *reinterpret_cast<const f32x4*>(lrec + REC / 4);
          const float m1 = ldsf[(NW + 1) * REC], m2 = ldsf[(NW + 2) * REC], mm = fmaxf(m1, m2);
          const float e1 = __builtin_amdgcn_exp2f(m1 - mm), e2 = __builtin_amdgcn_exp2f(m2 - mm);
          rec2 = rec1 * e1 + rec2 * e2;
          if (lane == 0) rec2[0] = mm;
        }
        __builtin_amdgcn_raw_buffer_store_b128(__builtin_bit_cast(u32x4, rec2), wrs, loff, (unsigned)rfl(dbh) * SLOT_BYTES, AUX_SC1);
        asm volatile("s_waitcnt vmcnt(0)" ::: "memory");
        if (lane == 0) __hip_atomic_store(reinterpret_cast<u64*>(ws + (size_t)dbh * SLOT_BYTES + SLOT_FLAG), ftag + 1u, RLX_AGENT);
        send = false;
      }
      if (recv && prog >= n0 + give - g1n + PWAIT) {
        const u64 fl = __hip_atomic_load(reinterpret_cast<u64*>(ws + (size_t)bh * SLOT_BYTES + SLOT_FLAG), RLX_AGENT);
        if (__all(fl == rtag + 1u)) {
          __builtin_amdgcn_fence(__ATOMIC_ACQUIRE, "agent");
          asm volatile("" ::: "memory");
          const u32x4 pay = __builtin_amdgcn_raw_buffer_load_b128(wrs, loff, (unsigned)bh * SLOT_BYTES, AUX_SC1);
          if (lane < REC / 4) *lrec = pay;
          asm volatile("s_waitcnt vmcnt(0)" ::: "memory");
          if (lane == 0) __hip_atomic_store(reinterpret_cast<u64*>(ws + (size_t)bh * SLOT_BYTES + SLOT_EPOCH), rtag + 1u, RLX_AGENT);
          recv = false;
        } else if (spins > (1u << 23)) {
          *lrec = u32x4{0u, 0x7fc00000u, 0u, 0u};
          recv = false;
        }
      }
      __builtin_amdgcn_s_sleep(6);
    }
  }
  float* rec = ldsf + wave * REC;
  *reinterpret_cast<f32x2*>(rec + 4 + d0) = f32x2{a0, a1};
  if (lane == 0) *reinterpret_cast<f32x2*>(rec) = f32x2{M, L};
  asm volatile("" :: "v"(cpf), "v"(wsl));
  __syncthreads();
  if (tid < 64) {
    float mw[NW + 1], lw[NW + 1], MM = -1e30f;
#pragma unroll
    for (int w = 0; w <= NW; ++w) {
      const f32x2 ml = *reinterpret_cast<const f32x2*>(ldsf + w * REC);
      mw[w] = ml[0]; lw[w] = ml[1];
      MM = fmaxf(MM, mw[w]);
    }
    float LL = 0.f, o0 = 0.f, o1 = 0.f;
#pragma unroll
    for (int w = 0; w <= NW; ++w) {
      const float s = __builtin_amdgcn_exp2f(mw[w] - MM);
      const f32x2 a = *reinterpret_cast<const f32x2*>(ldsf + w * REC + 4 + 2 * tid);
      LL = fmaf(s, lw[w], LL); o0 = fmaf(s, a[0], o0); o1 = fmaf(s, a[1], o1);
    }
    const float inv = __builtin_amdgcn_rcpf(LL);
    const __hip_bfloat16 r0 = __float2bfloat16(o0 * inv), r1 = __float2bfloat16(o1 * inv);
    const unsigned pk = (unsigned)__builtin_bit_cast(unsigned short, r0) | ((unsigned)__builtin_bit_cast(unsigned short, r1) << 16);
    asm volatile(".Lcpf_end:");
    *reinterpret_cast<unsigned*>(out + (size_t)bh * D + 2 * tid) = pk;
  }
}
}

extern "C" void kernel_launch(void* const* d_in, const int* in_sizes, int n_in, void* d_out, int out_size,
                              void* d_ws, size_t ws_size, hipStream_t stream) {
  const unsigned kv_bytes = (unsigned)in_sizes[1] * 2u;
  int dev = 0, khz = 0;
  if (hipGetDevice(&dev) != hipSuccess || hipDeviceGetAttribute(&khz, hipDeviceAttributeWallClockRate, dev) != hipSuccess) khz = 0;
  const float page_ticks = (float)(2.0 * PAGE_BYTES / PACE_GBS * 1e-6 * khz);
  paged_attn_kernel<<<B * H, NW * 64, 0, stream>>>(
      (const unsigned short*)d_in[0], (const unsigned short*)d_in[1], (const unsigned short*)d_in[2],
      (const int*)d_in[3], (const int*)d_in[4], (unsigned short*)d_out, (unsigned char*)d_ws, kv_bytes, page_ticks);
}
```

```cpp
#include <hip/hip_runtime.h>
#include <hip/hip_bf16.h>
#include <stdint.h>

namespace {
constexpr int B = 8, H = 32, D = 128, PAGE = 16, NBPS = 128, SKV = NBPS * PAGE;
constexpr int NS = 3, NW = NS + 1;
constexpr int K0 = 4;
constexpr int HX = 2;
constexpr int FMAXN = 48, FSPLIT = 12;
constexpr int PWAIT = 6;
constexpr double PACE_GBS = 7750;
constexpr double PACE_T0_NS = 500;
constexpr int AUX_NT = 2, AUX_SC1 = 16;
constexpr unsigned TOK_BYTES = H * D * 2;
constexpr unsigned PAGE_BYTES = PAGE * TOK_BYTES;
constexpr int REC = D + 4;
constexpr unsigned SLOT_BYTES = 1024, SLOT_FLAG = 544, SLOT_EPOCH = 576, SLOT_USED = 640;
constexpr int L_PROG = (NW + 3) * REC, L_FLAG = L_PROG + 1, L_SEND = L_PROG + 2, L_RNG = L_PROG + 4, L_FP = L_RNG + 4 * NS,
              L_FTAB = L_FP + 4, L_QF = L_FTAB + NBPS, L_SC = L_QF + 64, L_END = L_SC + NW * 64 * PAGE;

using bf16x2 = __attribute__((ext_vector_type(2))) __bf16;
using u32x4 = __attribute__((ext_vector_type(4))) unsigned;
using i32x4 = __attribute__((ext_vector_type(4))) int;
using f32x2 = __attribute__((ext_vector_type(2))) float;
using f32x4 = __attribute__((ext_vector_type(4))) float;
typedef unsigned long long u64;
#define RLX_AGENT __ATOMIC_RELAXED, __HIP_MEMORY_SCOPE_AGENT
#define RLX_WG __ATOMIC_RELAXED, __HIP_MEMORY_SCOPE_WORKGROUP
#define SB() __builtin_amdgcn_sched_barrier(0)

__device__ __forceinline__ float dot2(unsigned k, unsigned q, float acc) {
  return __builtin_amdgcn_fdot2_f32_bf16(__builtin_bit_cast(bf16x2, k), __builtin_bit_cast(bf16x2, q), acc, false);
}
template <int CTRL> __device__ __forceinline__ float dpp(float x) {
  return __builtin_bit_cast(float, __builtin_amdgcn_mov_dpp(__builtin_bit_cast(int, x), CTRL, 0xf, 0xf, true));
}
template <int CTRL> __device__ __forceinline__ int dppi(int x) { return __builtin_amdgcn_mov_dpp(x, CTRL, 0xf, 0xf, true); }
__device__ __forceinline__ float row_sum(float x) {
  x += dpp<0x128>(x);
  x += dpp<0x124>(x);
  x += dpp<0x122>(x);
  x += dpp<0x121>(x);
  return x;
}
__device__ __forceinline__ float xrow_max(float x) {
  auto s = __builtin_amdgcn_permlane16_swap(__float_as_uint(x), __float_as_uint(x), false, false);
  x = fmaxf(__uint_as_float(s[0]), __uint_as_float(s[1]));
  auto t = __builtin_amdgcn_permlane32_swap(__float_as_uint(x), __float_as_uint(x), false, false);
  return fmaxf(__uint_as_float(t[0]), __uint_as_float(t[1]));
}
__device__ __forceinline__ float xrow_sum(float x) {
  auto s = __builtin_amdgcn_permlane16_swap(__float_as_uint(x), __float_as_uint(x), false, false);
  x = __uint_as_float(s[0]) + __uint_as_float(s[1]);
  auto t = __builtin_amdgcn_permlane32_swap(__float_as_uint(x), __float_as_uint(x), false, false);
  return __uint_as_float(t[0]) + __uint_as_float(t[1]);
}
__device__ __forceinline__ int rfl(int x) { return __builtin_amdgcn_readfirstlane(x); }

__global__ void __launch_bounds__(NW * 64, 1)
paged_attn_kernel(const unsigned short* __restrict__ q, const unsigned short* __restrict__ kc,
                  const unsigned short* __restrict__ vc, const int* __restrict__ bt,
                  const int* __restrict__ lens, unsigned short* __restrict__ out, unsigned char* ws, unsigned kv_bytes, float page_ticks) {
  __shared__ __attribute__((aligned(16))) int lds[L_END];
  unsigned cpf;
  {
    u64 pc; unsigned off = (threadIdx.x & 63u) * 128u;
    asm volatile("s_getpc_b64 %0\n.Lcpf_here:\n\tv_min_u32 %1, .Lcpf_end-.Lcpf_here, %1" : "=s"(pc), "+v"(off));
    cpf = *reinterpret_cast<const __attribute__((address_space(1))) unsigned*>(pc + off);
    asm volatile("" ::: "memory");
  }
  const int bh = blockIdx.x, b = bh >> 5, h = bh & 31;
  const int tid = threadIdx.x, lane = tid & 63, r = lane >> 4, c = lane & 15;
  const int wave = rfl(tid >> 6);
  constexpr float C2 = 0.088388347648318440f * 1.4426950408889634f;

  asm volatile("" : "+s"(kv_bytes), "+s"(page_ticks) : "s"(vc), "s"(out), "s"(ws));
  int len = lens[b];
  const int phys0 = bt[b * NBPS + wave], phys1 = bt[b * NBPS + NS + wave];
  SB();
  const unsigned wsl = __hip_atomic_load(reinterpret_cast<const unsigned*>(ws + (size_t)bh * SLOT_BYTES + min(lane * 64u, SLOT_USED - 64u)), RLX_AGENT);
  const int bt0 = bt[b * NBPS + lane], bt1 = bt[b * NBPS + 64 + lane];
  const u32x4 qown = __builtin_nontemporal_load(reinterpret_cast<const u32x4*>(q + (size_t)bh * D + 8 * c));
  int lv = lens[lane & (B - 1)];
  if (tid == 0) *reinterpret_cast<i32x4*>(lds + L_PROG) = i32x4{0, 0, 0, 0};
  unsigned vb[4]; int rt[4];
#pragma unroll
  for (int t = 0; t < 4; ++t) {
    rt[t] = r + 4 * t; vb[t] = (unsigned)rt[t] * TOK_BYTES + (unsigned)c * 16u;
    asm volatile("" : "+v"(vb[t]), "+v"(rt[t]));
  }
  SB();
  const bool allm = len <= 0;
  len = allm ? SKV : min(len, SKV);
  u32x4 Ab[4], Bb[4];
#define SLOT(phys_, h_, rem_) ((unsigned)(phys_) * PAGE_BYTES + (unsigned)(h_) * (D * 2) + (unsigned)max(0, min((rem_), PAGE)))
#define RSRC(base_, t_) __builtin_amdgcn_make_buffer_rsrc((void*)(base_), 0, min(((t_) & ~(PAGE_BYTES - 1u)) + ((t_) & 31u) * TOK_BYTES, kv_bytes), 0x00020000)
#define ISSUE(X, rs, so_) do { _Pragma("unroll") for (int t_ = 0; t_ < 4; ++t_) X[t_] = __builtin_amdgcn_raw_buffer_load_b128(rs, vb[t_], so_, AUX_NT); } while (0)
  unsigned tA = SLOT(phys0, h, wave < NS ? len - wave * PAGE : 0), tB = SLOT(phys1, h, wave < NS ? len - (NS + wave) * PAGE : 0);
  ISSUE(Ab, RSRC(kc, tA), tA & ~255u); SB();
  ISSUE(Bb, RSRC(kc, tB), tB & ~255u); SB();
  const unsigned t_ref = (unsigned)__builtin_amdgcn_s_memrealtime();
  asm volatile("s_waitcnt lgkmcnt(0)\n\ts_barrier" ::: "memory");

  const int np = (len + PAGE - 1) / PAGE;
  const int d0 = 8 * c + 2 * r;
  int pnext = 0, pend = 0;
  int give = 0, F = 0;
  int dbh = 0, f1n = 0, g1n = 0, n0 = 0;
  u64 rtag = 0, ftag = 0;
  if (wave == NS) {
    const int sj = lane & (B - 1);
    rtag = __hip_atomic_load(reinterpret_cast<u64*>(ws + (size_t)bh * SLOT_BYTES + SLOT_EPOCH), RLX_AGENT);
    const u64 allmask = __ballot(lv <= 0);
    lv = lv <= 0 ? SKV : min(lv, SKV);
    const int npv = (lv + PAGE - 1) / PAGE;
    const int key = lv * B + (B - 1 - sj);
    int rank = 0;
#pragma unroll
    for (int j = 0; j < B; ++j) rank += __builtin_amdgcn_readlane(key, j) > key ? 1 : 0;
    int tot = npv + dppi<0x124>(npv);
    tot += dppi<0x122>(tot);
    tot += dppi<0x121>(tot);
    const int pos = rank < B / 2 ? B - 2 - 2 * rank : 2 * rank - B + 1;
    const int mypos = __builtin_amdgcn_readlane(pos, b);
    const int pb = __builtin_ctzll(__ballot(pos == ((mypos + B - 1) & (B - 1))));
    dbh = pb * H + (h ^ HX);
    const int f0 = bt[pb * NBPS + lane], f1 = bt[pb * NBPS + 64 + lane];
    int qf = __builtin_nontemporal_load(reinterpret_cast<const int*>(q) + dbh * (D / 2) + lane);
    ftag = __hip_atomic_load(reinterpret_cast<u64*>(ws + (size_t)dbh * SLOT_BYTES + SLOT_EPOCH), RLX_AGENT);
    const u64 fold = __hip_atomic_load(reinterpret_cast<u64*>(ws + (size_t)dbh * SLOT_BYTES + SLOT_FLAG), RLX_AGENT);
    const int ex = npv - (tot / B + (pos < tot % B ? 1 : 0));
    const int pk = (ex << 3) | pos;
    int pre = 0;
#pragma unroll
    for (int j = 0; j < B; ++j) { const int pj = __builtin_amdgcn_readlane(pk, j); pre += (pj & 7) <= pos ? pj >> 3 : 0; }
    int pmin = min(pre, dppi<0x124>(pre));
    pmin = min(pmin, dppi<0x122>(pmin));
    pmin = min(pmin, dppi<0x121>(pmin));
    const int gv = max(0, min(min(pre - pmin, tot * FMAXN / (32 * B * NS)), npv - K0 * NS));
    give = __builtin_amdgcn_readlane(gv, b);
    F = __builtin_amdgcn_readlane(gv, pb);
    const int lo = min(np, K0 * NS), own = np - give - lo;
    const int top = max(0, min(own + F + lo - (rfl(tot) + B * NS * 2 - 1) / (B * NS * 2) * (NS * 2), (own + F + lo) / NW));
    const int S = min(top, own), rest = own - S;
    const int pl = __builtin_amdgcn_readlane(lv, pb), pnp = (pl + PAGE - 1) / PAGE;
    f1n = F * (32 * B * NS) > rfl(tot) * FSPLIT ? F / 2 : 0;
    g1n = give * (32 * B * NS) > rfl(tot) * FSPLIT ? give / 2 : 0;
    const int c2 = max(0, min((rest + F + 1) / (2 * NS) * 2 - (F - f1n), rest));
    const int c1 = max(0, min((rest + F + 3) / (2 * NS) * 2 - f1n, rest - c2)), c0 = rest - c1 - c2;
    if (lane == 0) {
      *reinterpret_cast<i32x4*>(lds + L_RNG) = i32x4{lo, lo + c0, 0, 0};
      *reinterpret_cast<i32x4*>(lds + L_RNG + 4) = i32x4{lo + c0, lo + c0 + c1, pnp - F, pnp - F + f1n};
      *reinterpret_cast<i32x4*>(lds + L_RNG + 8) = i32x4{lo + c0 + c1, lo + rest, pnp - F + f1n, pnp};
      lds[L_FP] = pl; lds[L_FP + 1] = tot;
      asm volatile("s_waitcnt lgkmcnt(0)" ::: "memory");
      __hip_atomic_store(lds + L_FLAG, 1, RLX_WG);
    }
    pnext = rfl(lo + rest); pend = rfl(pnext + S); n0 = K0 + c0;
    if ((allmask >> pb) & 1) qf = 0;
    lds[L_FTAB + lane] = f0; lds[L_FTAB + 64 + lane] = f1; lds[L_QF + lane] = qf;
    asm volatile("s_waitcnt lgkmcnt(0)" ::: "memory");
    if (lane == 0) {
      __hip_atomic_store(lds + L_FLAG, 2, RLX_WG);
      if (fold == ftag + 1u) __hip_atomic_store(reinterpret_cast<u64*>(ws + (size_t)dbh * SLOT_BYTES + SLOT_FLAG), ftag, RLX_AGENT);
    }
    __builtin_amdgcn_s_waitcnt(0x0F70);
  }

  float M = -1e30f, L = 0.f, a0 = 0.f, a1 = 0.f;
  float* ldsf = reinterpret_cast<float*>(lds);
  if (wave < NS || pnext < pend) {
    u32x4 qv = allm ? u32x4{0u, 0u, 0u, 0u} : qown;
    float m = -1e30f, mo = -1e30f, l = 0.f;
    f32x2 acc2[4];
#pragma unroll
    for (int k = 0; k < 4; ++k) acc2[k] = f32x2{0.f, 0.f};
    if (wave >= NS)
      while (rfl(__hip_atomic_load(lds + L_PROG, RLX_WG)) < n0 - (pend - pnext) - 2) __builtin_amdgcn_s_sleep(8);
    int tau = 0, now = 0;
    const int t0 = rfl((int)(page_ticks * (float)(PACE_T0_NS * PACE_GBS / (2.0 * PAGE_BYTES))));
#define PACE(i_) do { const int due = t0 + (((NS * (i_) + wave) * tau) >> 8); \
      if (now < due) while ((now = (int)((unsigned)__builtin_amdgcn_s_memrealtime() - t_ref)) < due) __builtin_amdgcn_s_sleep(1); } while (0)
    int fst = 0, cf = 0, no = 64, n = 64, sfill = -1;
    unsigned tK = SLOT(__builtin_amdgcn_ds_bpermute((lane * NS + wave) << 2, bt0), h, lane < K0 && wave < NS ? len - (lane * NS + wave) * PAGE : 0), tV = 0;
    f32x4* sc = reinterpret_cast<f32x4*>(ldsf + L_SC + wave * 64 * PAGE) + r;
#define KSTEP(X, tX, s_) do { \
      const unsigned tn = (s_) + 2 < n ? __builtin_amdgcn_readlane(tK, (s_) + 2) : __builtin_amdgcn_readlane(tV, (s_) + 2 - n); \
      const __amdgpu_buffer_rsrc_t nrs = RSRC((s_) + 2 < n ? kc : vc, tn); \
      if ((s_) == no) { mo = m; m = -1e30f; qv = *reinterpret_cast<const u32x4*>(lds + L_QF + 4 * c); } \
      SB(); \
      float x[4]; \
      _Pragma("unroll") for (int t = 0; t < 4; ++t) { \
        float d = 0.f; \
        _Pragma("unroll") for (int k = 0; k < 4; ++k) d = dot2(X[t][k], qv[k], d); \
        x[t] = d; \
        asm volatile("" : "+v"(x[t]) :: "memory"); \
        SB(); \
        X[t] = __builtin_amdgcn_raw_buffer_load_b128(nrs, vb[t], tn & ~255u, AUX_NT); \
        asm volatile("" ::: "memory"); \
        SB(); \
      } \
      _Pragma("unroll") for (int t = 0; t < 4; ++t) { \
        x[t] = row_sum(x[t]) * C2; \
        x[t] = (rt[t] < (int)(tX & 31u)) ? x[t] : -INFINITY; \
        m = fmaxf(m, x[t]); \
      } \
      if (c == 0) sc[4 * (s_)] = f32x4{x[0], x[1], x[2], x[3]}; \
      tX = tn; \
    } while (0)
#define VSTEP(X, j_) do { \
      const unsigned tn = __builtin_amdgcn_readlane(tV, (j_) + 2); \
      const __amdgpu_buffer_rsrc_t nrs = RSRC(vc, tn); \
      const f32x4 x = sc[4 * ((j_) < cf ? no + (j_) : (j_) - cf)]; \
      float p[4]; \
      _Pragma("unroll") for (int t = 0; t < 4; ++t) p[t] = __builtin_amdgcn_exp2f(x[t] - m); \
      l += (p[0] + p[1]) + (p[2] + p[3]); \
      SB(); \
      _Pragma("unroll") for (int t = 0; t < 4; ++t) { \
        _Pragma("unroll") for (int k = 0; k < 4; ++k) { \
          const unsigned w = X[t][k]; \
          const f32x2 v2 = {__builtin_bit_cast(float, w << 16), __builtin_bit_cast(float, w & 0xffff0000u)}; \
          acc2[k] = __builtin_elementwise_fma(f32x2{p[t], p[t]}, v2, acc2[k]); \
        } \
        asm volatile("" : "+v"(acc2[0]), "+v"(acc2[1]), "+v"(acc2[2]), "+v"(acc2[3]) :: "memory"); \
        SB(); \
        X[t] = __builtin_amdgcn_raw_buffer_load_b128(nrs, vb[t], tn & ~255u, AUX_NT); \
        asm volatile("" ::: "memory"); \
        SB(); \
      } \
    } while (0)
#define FLUSH() do { \
      M = m; \
      L = xrow_sum(l); \
      float s4[4]; \
      _Pragma("unroll") for (int k = 0; k < 4; ++k) {       \
        auto w_ = __builtin_amdgcn_permlane32_swap(__float_as_uint(acc2[k >> 1][k & 1]), __float_as_uint(acc2[2 + (k >> 1)][k & 1]), false, false); \
        s4[k] = __uint_as_float(w_[0]) + __uint_as_float(w_[1]); } \
      auto u0_ = __builtin_amdgcn_permlane16_swap(__float_as_uint(s4[0]), __float_as_uint(s4[2]), false, false); \
      auto u1_ = __builtin_amdgcn_permlane16_swap(__float_as_uint(s4[1]), __float_as_uint(s4[3]), false, false); \
      a0 = __uint_as_float(u0_[0]) + __uint_as_float(u0_[1]); \
      a1 = __uint_as_float(u1_[0]) + __uint_as_float(u1_[1]); } while (0)
#define PUBLISH() do { \
      float* srec = ldsf + (NW + wave) * REC; \
      *reinterpret_cast<f32x2*>(srec + 4 + d0) = f32x2{a0, a1}; \
      if (lane == 0) *reinterpret_cast<f32x2*>(srec) = f32x2{M, L}; \
      asm volatile("s_waitcnt lgkmcnt(0)" ::: "memory"); \
      if (lane == 0) __hip_atomic_store(lds + L_SEND + wave - 1, 1, RLX_WG); \
      m = mo; l = 0.f; \
      _Pragma("unroll") for (int k = 0; k < 4; ++k) acc2[k] = f32x2{0.f, 0.f}; } while (0)
    for (int s = 0; s < n; s += 2) {
      s = rfl(s);
      if (tid == 0) __hip_atomic_store(lds + L_PROG, s, RLX_WG);
      if (s == K0 - 2) {
        if (wave < NS) {
          while (rfl(__hip_atomic_load(lds + L_FLAG, RLX_WG)) < 1) __builtin_amdgcn_s_sleep(1);
          asm volatile("" ::: "memory");
          const i32x4 rg = *reinterpret_cast<const i32x4*>(lds + L_RNG + 4 * wave);
          pnext = rfl(rg[0]); pend = rfl(rg[1]); fst = rfl(rg[2]); cf = rfl(rg[3]) - fst;
        }
        no = K0 + pend - pnext + ((pend - pnext + cf) & 1); n = no + cf;
        sfill = no >= K0 + 2 ? K0 : K0 - 2;
        if (wave < NS) tau = rfl((int)((float)lds[L_FP + 1] * page_ticks * (256.f / (2 * NS)) * __builtin_amdgcn_rcpf((float)n)));
        const int pn = pnext + lane - K0;
        const int p0 = __builtin_amdgcn_ds_bpermute(pn << 2, bt0), p1 = __builtin_amdgcn_ds_bpermute(pn << 2, bt1);
        if (lane >= K0) tK = pn < pend ? SLOT((pn & 64) ? p1 : p0, h, len - pn * PAGE) : 0u;
        const bool far = lane >= K0 && tK >= (0x80000000u >> __builtin_clz(kv_bytes));
        const u64 fm = __ballot(far);
        if (fm) {
          const int below = __builtin_amdgcn_mbcnt_hi((unsigned)(fm >> 32), __builtin_amdgcn_mbcnt_lo((unsigned)fm, 0u));
          const int dst = far ? K0 + below : lane < K0 ? lane : lane + __popcll(fm) - below;
          tK = __builtin_amdgcn_ds_permute(dst << 2, tK);
        }
      }
      if (s == sfill) {
        if (cf > 0) {
          while (rfl(__hip_atomic_load(lds + L_FLAG, RLX_WG)) < 2) __builtin_amdgcn_s_sleep(1);
          asm volatile("" ::: "memory");
          const int pn = fst + lane - no;
          if (lane >= no && lane < n) tK = SLOT(lds[L_FTAB + pn], h ^ HX, lds[L_FP] - pn * PAGE);
        }
        tV = __builtin_amdgcn_ds_bpermute((lane < cf ? no + lane : lane < n ? lane - cf : 63) << 2, tK);
      }
      PACE(s);
      KSTEP(Ab, tA, s);
      PACE(s + 1);
      KSTEP(Bb, tB, s + 1);
    }
    m = xrow_max(m); mo = xrow_max(mo);
    for (int j = 0;; j += 2) {
      j = rfl(j);
      if (tid == 0) __hip_atomic_store(lds + L_PROG, n + j, RLX_WG);
      if (j + 2 < n) PACE(n + j);
      VSTEP(Ab, j);
      if (__builtin_expect(j + 1 == cf, 0)) { FLUSH(); PUBLISH(); }
      if (j + 2 < n) PACE(n + j + 1);
      VSTEP(Bb, j + 1);
      const bool fin = j + 2 == n;
      if (j + 2 == cf || fin) {
        FLUSH();
        if (fin) break;
        PUBLISH();
      }
    }
    if (tid == 0) __hip_atomic_store(lds + L_PROG, 1 << 20, RLX_WG);
  }
  if (wave == NS) {
    const __amdgpu_buffer_rsrc_t wrs = __builtin_amdgcn_make_buffer_rsrc(ws, 0, B * H * SLOT_BYTES, 0x00020000);
    const unsigned loff = lane < REC / 4 ? lane * 16u : 0x80000000u;
    u32x4* lrec = reinterpret_cast<u32x4*>(lds + NW * REC) + min(lane, REC / 4 - 1);
    if (give <= 0) *lrec = u32x4{lane ? 0u : __float_as_uint(-1e30f), 0u, 0u, 0u};
    bool send = F > 0, recv = give > 0;
    for (unsigned spins = 0; send || recv; ++spins) {
      const int prog = rfl(__hip_atomic_load(lds + L_PROG, RLX_WG));
      const int s1 = rfl(__hip_atomic_load(lds + L_SEND, RLX_WG)), s2 = rfl(__hip_atomic_load(lds + L_SEND + 1, RLX_WG));
      if (send && s2 && (s1 || f1n == 0)) {
        asm volatile("" ::: "memory");
        f32x4 rec2 = *reinterpret_cast<const f32x4*>(lrec + 2 * (REC / 4));
        if (f1n) {
          const f32x4 rec1 = *reinterpret_cast<const f32x4*>(lrec + REC / 4);
          const float m1 = ldsf[(NW + 1) * REC], m2 = ldsf[(NW + 2) * REC], mm = fmaxf(m1, m2);
          const float e1 = __builtin_amdgcn_exp2f(m1 - mm), e2 = __builtin_amdgcn_exp2f(m2 - mm);
          rec2 = rec1 * e1 + rec2 * e2;
          if (lane == 0) rec2[0] = mm;
        }
        __builtin_amdgcn_raw_buffer_store_b128(__builtin_bit_cast(u32x4, rec2), wrs, loff, (unsigned)rfl(dbh) * SLOT_BYTES, AUX_SC1);
        asm volatile("s_waitcnt vmcnt(0)" ::: "memory");
        if (lane == 0) __hip_atomic_store(reinterpret_cast<u64*>(ws + (size_t)dbh * SLOT_BYTES + SLOT_FLAG), ftag + 1u, RLX_AGENT);
        send = false;
      }
      if (recv && prog >= n0 + give - g1n + PWAIT) {
        const u64 fl = __hip_atomic_load(reinterpret_cast<u64*>(ws + (size_t)bh * SLOT_BYTES + SLOT_FLAG), RLX_AGENT);
        if (__all(fl == rtag + 1u)) {
          __builtin_amdgcn_fence(__ATOMIC_ACQUIRE, "agent");
          asm volatile("" ::: "memory");
          const u32x4 pay = __builtin_amdgcn_raw_buffer_load_b128(wrs, loff, (unsigned)bh * SLOT_BYTES, AUX_SC1);
          if (lane < REC / 4) *lrec = pay;
          asm volatile("s_waitcnt vmcnt(0)" ::: "memory");
          if (lane == 0) __hip_atomic_store(reinterpret_cast<u64*>(ws + (size_t)bh * SLOT_BYTES + SLOT_EPOCH), rtag + 1u, RLX_AGENT);
          recv = false;
        } else if (spins > (1u << 23)) {
          *lrec = u32x4{0u, 0x7fc00000u, 0u, 0u};
          recv = false;
        }
      }
      __builtin_amdgcn_s_sleep(6);
    }
  }
  float* rec = ldsf + wave * REC;
  *reinterpret_cast<f32x2*>(rec + 4 + d0) = f32x2{a0, a1};
  if (lane == 0) *reinterpret_cast<f32x2*>(rec) = f32x2{M, L};
  asm volatile("" :: "v"(cpf), "v"(wsl));
  __syncthreads();
  if (tid < 64) {
    float mw[NW + 1], lw[NW + 1], MM = -1e30f;
#pragma unroll
    for (int w = 0; w <= NW; ++w) {
      const f32x2 ml = *reinterpret_cast<const f32x2*>(ldsf + w * REC);
      mw[w] = ml[0]; lw[w] = ml[1];
      MM = fmaxf(MM, mw[w]);
    }
    float LL = 0.f, o0 = 0.f, o1 = 0.f;
#pragma unroll
    for (int w = 0; w <= NW; ++w) {
      const float s = __builtin_amdgcn_exp2f(mw[w] - MM);
      const f32x2 a = *reinterpret_cast<const f32x2*>(ldsf + w * REC + 4 + 2 * tid);
      LL = fmaf(s, lw[w], LL); o0 = fmaf(s, a[0], o0); o1 = fmaf(s, a[1], o1);
    }
    const float inv = __builtin_amdgcn_rcpf(LL);
    const __hip_bfloat16 r0 = __float2bfloat16(o0 * inv), r1 = __float2bfloat16(o1 * inv);
    const unsigned pk = (unsigned)__builtin_bit_cast(unsigned short, r0) | ((unsigned)__builtin_bit_cast(unsigned short, r1) << 16);
    asm volatile(".Lcpf_end:");
    *reinterpret_cast<unsigned*>(out + (size_t)bh * D + 2 * tid) = pk;
  }
}
}

extern "C" void kernel_launch(void* const* d_in, const int* in_sizes, int n_in, void* d_out, int out_size,
                              void* d_ws, size_t ws_size, hipStream_t stream) {
  const unsigned kv_bytes = (unsigned)in_sizes[1] * 2u;
  int dev = 0, khz = 0;
  if (hipGetDevice(&dev) != hipSuccess || hipDeviceGetAttribute(&khz, hipDeviceAttributeWallClockRate, dev) != hipSuccess) khz = 0;
  const float page_ticks = (float)(2.0 * PAGE_BYTES / PACE_GBS * 1e-6 * khz);
  paged_attn_kernel<<<B * H, NW * 64, 0, stream>>>(
      (const unsigned short*)d_in[0], (const unsigned short*)d_in[1], (const unsigned short*)d_in[2],
      (const int*)d_in[3], (const int*)d_in[4], (unsigned short*)d_out, (unsigned char*)d_ws, kv_bytes, page_ticks);
}
```
